```python
import torch

def _softmax(x, axis=-1):
    return torch.softmax(x, dim=axis)

def _gelu(x):
    return torch.nn.functional.gelu(x, approximate='tanh')

def _silu(x):
    return torch.nn.functional.silu(x)

def _sigmoid(x):
    return torch.sigmoid(x)

def _layernorm(x, axis=-1, eps=1e-05):
    m = x.mean(dim=axis, keepdim=True)
    v = x.var(dim=axis, keepdim=True, unbiased=False)
    return (x - m) / torch.sqrt(v + eps)

def _scatter_add(tgt, idx, src, axis=0):
    return tgt.clone().index_add_(axis, idx, src)

def _segment_sum(src, seg, n):
    out = torch.zeros((n,) + src.shape[1:], dtype=src.dtype)
    return out.index_add_(0, seg, src)

def _pad(x, pw):
    flat = tuple((v for pair in reversed(pw) for v in pair))
    return torch.nn.functional.pad(x, flat)
kernel_name = 'gated_mix__mha_block__memory_aug_recurrent_mem_tr__prompt'

def setup_inputs():

    _w = lambda a: (lambda t: t.to(torch.bfloat16) if t.is_floating_point() else t)(torch.as_tensor(a))

    def _A_setup():
        B, S, D, H = (1, 2048, 640, 32)
        return {'x': _w((lambda *s: torch.randn(*s, dtype=torch.bfloat16) * 0.05)(B, S, D)), 'wqkv': _w((lambda *s: torch.randn(*s, dtype=torch.bfloat16) * 0.8)(D, 3 * D)), 'wo': _w((lambda *s: torch.randn(*s, dtype=torch.bfloat16) * 0.05)(D, D))}
    _A_in = _A_setup()

    def _B_setup():
        B, S, Dm = (1, 2048, 640)
        H, Dh = (32, 128)
        M, seg = (8, 32)
        seg = min(seg, S)
        S = S // seg * seg
        return {'x': _w((lambda *s: torch.randn(*s, dtype=torch.bfloat16) * 0.05)(B, S, Dm)), 'mem0': _w((lambda *s: torch.randn(*s, dtype=torch.bfloat16) * 0.8)(B, M, Dm)), 'Wq': _w((lambda *s: torch.randn(*s, dtype=torch.bfloat16) * 3.2)(Dm, H, Dh) * Dm ** (-0.5)), 'Wk': _w((lambda *s: torch.randn(*s, dtype=torch.bfloat16) * 3.2)(Dm, H, Dh) * Dm ** (-0.5)), 'Wv': _w((lambda *s: torch.randn(*s, dtype=torch.bfloat16) * 0.8)(Dm, H, Dh) * Dm ** (-0.5)), 'Wo': _w((lambda *s: torch.randn(*s, dtype=torch.bfloat16) * 0.8)(H, Dh, Dm) * (H * Dh) ** (-0.5)), 'seg_len': seg}
    _B_in = _B_setup()
    return {'A_x': _A_in['x'], 'A_wqkv': _A_in['wqkv'], 'A_wo': _A_in['wo'], 'B_x': _B_in['x'], 'B_mem0': _B_in['mem0'], 'B_Wq': _B_in['Wq'], 'B_Wk': _B_in['Wk'], 'B_Wv': _B_in['Wv'], 'B_Wo': _B_in['Wo'], 'B_seg_len': _B_in['seg_len'], 'gate_w': _w((lambda *s: torch.randn(*s, dtype=torch.bfloat16) * 0.05)(640) * 16.0)}

def reference(A_x, A_wqkv, A_wo, B_x, B_mem0, B_Wq, B_Wk, B_Wv, B_Wo, B_seg_len, gate_w):

    def _A_ref(x, wqkv, wo):
        B, S, D = x.shape
        H = 32
        Dh = D // H
        qkv = (lambda x, *d: x.permute(*d))((x @ wqkv).reshape(B, S, 3, H, Dh), 2, 0, 3, 1, 4)
        q, k, v = (qkv[0], qkv[1], qkv[2])
        scores = torch.einsum('bhqd,bhkd->bhqk', q, k) * Dh ** (-0.5)
        p = _softmax(scores, axis=-1)
        o = torch.einsum('bhqk,bhkd->bhqd', p, v)
        return (lambda x, *d: x.permute(*d))(o, 0, 2, 1, 3).reshape(B, S, D) @ wo

    def _B_ref(x, mem0, Wq, Wk, Wv, Wo, seg_len):
        B, S, Dm = x.shape
        H, Dh = (Wq.shape[1], Wq.shape[2])
        M = mem0.shape[1]
        scale = Dh ** (-0.5)
        n_seg = S // seg_len
        L = M + seg_len
        idx = torch.arange(L)
        mask = (idx[:, None] < M) | (idx[:, None] >= idx[None, :])
        mem = mem0
        outs = []
        for s in range(n_seg):
            xs = x[:, s * seg_len:(s + 1) * seg_len, :].clone()
            h = torch.concatenate([mem, xs], axis=1)
            q = torch.einsum('btd,dhe->bhte', h, Wq)
            k = torch.einsum('btd,dhe->bhte', h, Wk)
            v = torch.einsum('btd,dhe->bhte', h, Wv)
            scores = torch.einsum('bhqe,bhke->bhqk', q, k) * scale
            scores = torch.where(mask[None, None], scores, -1000000000.0)
            p = _softmax(scores, axis=-1)
            o = torch.einsum('bhqk,bhke->bhqe', p, v)
            o = torch.einsum('bhte,hed->btd', o, Wo)
            mem = o[:, :M, :].clone()
            outs.append(o[:, M:, :])
        return torch.concatenate(outs, axis=1)
    x = A_x
    g = _sigmoid((x * gate_w).sum(-1, keepdims=True))
    a = _A_ref(x, A_wqkv, A_wo)
    b = _B_ref(x, B_mem0, B_Wq, B_Wk, B_Wv, B_Wo, B_seg_len)
    return g * a + (1 - g) * b
```

```cpp
#include <hip/hip_runtime.h>
#include <hip/hip_bf16.h>
typedef __bf16 bf;
namespace nv {
__device__ __forceinline__ float rb(float x) { return (float)(bf)x; }
__global__ void gemm(const bf* A, int lda, const bf* B, int ldb, bf* C, int ldc, int M, int N, int K) {
  int n = blockIdx.x * 64 + (threadIdx.x & 63), m = blockIdx.y * 4 + (threadIdx.x >> 6);
  if (n >= N || m >= M) return;
  float acc = 0;
  for (int k = 0; k < K; k++) acc += (float)A[(size_t)m * lda + k] * (float)B[(size_t)k * ldb + n];
  C[(size_t)m * ldc + n] = (bf)acc;
}
__global__ void attnA(const bf* qkv, bf* o) {
  __shared__ float sc[2048]; __shared__ float red[64];
  int h = blockIdx.x, q = blockIdx.y, t = threadIdx.x;
  float qv[20]; for (int d = 0; d < 20; d++) qv[d] = (float)qkv[(size_t)q * 1920 + h * 20 + d];
  const float scale = 0.22360679774997896f;
  float mx = -1e30f;
  for (int k = t; k < 2048; k += 64) { float s = 0; for (int d = 0; d < 20; d++) s += qv[d] * (float)qkv[(size_t)k * 1920 + 640 + h * 20 + d]; s = rb(rb(s) * scale); sc[k] = s; mx = fmaxf(mx, s); }
  red[t] = mx; __syncthreads(); for (int i = 0; i < 64; i++) mx = fmaxf(mx, red[i]); __syncthreads();
  float sum = 0; for (int k = t; k < 2048; k += 64) { float e = expf(sc[k] - mx); sc[k] = e; sum += e; }
  red[t] = sum; __syncthreads(); sum = 0; for (int i = 0; i < 64; i++) sum += red[i];
  for (int k = t; k < 2048; k += 64) sc[k] = rb(sc[k] / sum);
  __syncthreads();
  if (t < 20) { float a = 0; for (int k = 0; k < 2048; k++) a += sc[k] * (float)qkv[(size_t)k * 1920 + 1280 + h * 20 + t]; o[(size_t)q * 640 + h * 20 + t] = (bf)a; }
}
__global__ void attnB(const bf* q, const bf* k, const bf* v, bf* o) {
  __shared__ float p[40];
  int h = blockIdx.x, r = blockIdx.y, t = threadIdx.x;
  const float scale = 0.08838834764831845f;
  if (t < 40) { float s = 0; for (int e = 0; e < 128; e++) s += (float)q[r * 4096 + h * 128 + e] * (float)k[t * 4096 + h * 128 + e]; s = rb(rb(s) * scale); if (!(r < 8 || r >= t)) s = -1e9f; p[t] = s; }
  __syncthreads();
  float mx = -1e30f; for (int j = 0; j < 40; j++) mx = fmaxf(mx, p[j]);
  float sum = 0; for (int j = 0; j < 40; j++) sum += expf(p[j] - mx);
  float a = 0; for (int j = 0; j < 40; j++) a += rb(expf(p[j] - mx) / sum) * (float)v[j * 4096 + h * 128 + t];
  o[r * 4096 + h * 128 + t] = (bf)a;
}
__global__ void fin(const bf* x, const bf* gw, const bf* a, const bf* b, bf* out) {
  __shared__ float red[64];
  int r = blockIdx.x, t = threadIdx.x;
  float s = 0; for (int d = t; d < 640; d += 64) s += rb((float)x[r * 640 + d] * (float)gw[d]);
  red[t] = s; __syncthreads(); s = 0; for (int i = 0; i < 64; i++) s += red[i];
  s = rb(s); float g = rb(1.f / (1.f + expf(-s))); float og = rb(1.f - g);
  for (int d = t; d < 640; d += 64) out[r * 640 + d] = (bf)(rb(g * (float)a[r * 640 + d]) + rb(og * (float)b[r * 640 + d]));
}
inline void run(void* const* d_in, bf* out, bf* ws, hipStream_t stream, bf* memdump = nullptr) {
  const bf *x = (const bf*)d_in[0], *wqkv = (const bf*)d_in[1], *wo = (const bf*)d_in[2], *mem0 = (const bf*)d_in[4], *Wq = (const bf*)d_in[5], *Wk = (const bf*)d_in[6], *Wv = (const bf*)d_in[7], *Wo = (const bf*)d_in[8], *gw = (const bf*)d_in[10];
  bf* qkv = ws; bf* oa = qkv + 2048 * 1920; bf* a = oa + 2048 * 640; bf* b = a + 2048 * 640; bf* hb = b + 2048 * 640; bf* q = hb + 40 * 640; bf* k = q + 40 * 4096; bf* v = k + 40 * 4096; bf* o = v + 40 * 4096; bf* oo = o + 40 * 4096;
  gemm<<<dim3(30, 512), 256, 0, stream>>>(x, 640, wqkv, 1920, qkv, 1920, 2048, 1920, 640);
  attnA<<<dim3(32, 2048), 64, 0, stream>>>(qkv, oa);
  gemm<<<dim3(10, 512), 256, 0, stream>>>(oa, 640, wo, 640, a, 640, 2048, 640, 640);
  hipMemcpyAsync(hb, mem0, 8 * 640 * 2, hipMemcpyDeviceToDevice, stream);
  for (int s = 0; s < 64; s++) {
    if (memdump) hipMemcpyAsync(memdump + s * 5120, hb, 8 * 640 * 2, hipMemcpyDeviceToDevice, stream);
    hipMemcpyAsync(hb + 8 * 640, x + s * 32 * 640, 32 * 640 * 2, hipMemcpyDeviceToDevice, stream);
    gemm<<<dim3(64, 10), 256, 0, stream>>>(hb, 640, Wq, 4096, q, 4096, 40, 4096, 640);
    gemm<<<dim3(64, 10), 256, 0, stream>>>(hb, 640, Wk, 4096, k, 4096, 40, 4096, 640);
    gemm<<<dim3(64, 10), 256, 0, stream>>>(hb, 640, Wv, 4096, v, 4096, 40, 4096, 640);
    attnB<<<dim3(32, 40), 128, 0, stream>>>(q, k, v, o);
    gemm<<<dim3(10, 10), 256, 0, stream>>>(o, 4096, Wo, 640, oo, 640, 40, 640, 4096);
    hipMemcpyAsync(hb, oo, 8 * 640 * 2, hipMemcpyDeviceToDevice, stream);
    hipMemcpyAsync(b + s * 32 * 640, oo + 8 * 640, 32 * 640 * 2, hipMemcpyDeviceToDevice, stream);
  }
  fin<<<2048, 64, 0, stream>>>(x, gw, a, b, out);
}
}
extern "C" void kernel_launch(void* const* d_in, const int* in_sizes, int n_in, void* d_out, int out_size, void* d_ws, size_t ws_size, hipStream_t stream) {
  nv::run(d_in, (bf*)d_out, (bf*)d_ws, stream);
}
```

```cpp
#include <hip/hip_runtime.h>
#include <hip/hip_bf16.h>

typedef __bf16 bf16;
typedef __attribute__((ext_vector_type(2))) __bf16 bf16x2;
typedef __attribute__((ext_vector_type(8))) __bf16 bf16x8;
typedef __attribute__((ext_vector_type(16))) __bf16 bf16x16;
typedef __attribute__((ext_vector_type(4))) short s16x4;
typedef __attribute__((ext_vector_type(2))) float f32x2;
typedef __attribute__((ext_vector_type(4))) float f32x4;
typedef __attribute__((ext_vector_type(16))) float f32x16;
typedef __attribute__((ext_vector_type(2))) unsigned u32x2;
typedef __attribute__((ext_vector_type(4))) unsigned u32x4;
typedef __attribute__((ext_vector_type(8))) unsigned u32x8;
typedef __amdgpu_buffer_rsrc_t rsrc_t;
#define DEV __device__ __forceinline__
#define SC1 16

#define ABL_S(s) (s)
#define ABL_PT(c) true
constexpr int NSEG = 64, NH = 32, DM = 640;
constexpr unsigned KB = 1024u, MB = 1024u * 1024u;
constexpr unsigned OFF_CTRL = 0;
constexpr unsigned OFF_GATE = 64 * KB;
constexpr unsigned OFF_XF = 1 * MB;
constexpr unsigned OFF_OF = 4 * MB;
constexpr unsigned OFF_WO = 10 * MB;
constexpr unsigned OFF_QA = 11 * MB;
constexpr unsigned OFF_KV = 14 * MB;
constexpr unsigned OFF_PT = 20 * MB;
constexpr unsigned OFF_VO = 46 * MB;
constexpr unsigned OFF_QKT = 72 * MB;
constexpr unsigned OFF_ZM = 104 * MB;
constexpr unsigned OFF_ZT = 124 * MB;
constexpr unsigned OFF_COMM = 204 * MB;
constexpr unsigned WS_USED = 220 * MB;
constexpr int C_TICKET = 0;
constexpr int C_OVF = 8, C_QKVDONE = 9, C_QATT = 10, C_QFIN = 11;
constexpr int C_VO = 12;
constexpr int C_AT = 16;
constexpr int C_POL = 24;
constexpr int C_PQK = 32;
constexpr int C_PVZ = 96;
constexpr int C_PCH = 160;
constexpr int C_LINES = 320;
constexpr unsigned SENT = 0xFFFFFFFFu;
constexpr int SPIN_CAP = 1 << 21;
constexpr int KVT = 2816, QAT = 1280, PTT = 1280;
constexpr unsigned CM_MS = 0, CM_P = 10240, CM_MX = 65536, CM_REP = 65536  ;
constexpr int NBLK = 10, SPP = 12, NPAGE = 6;
DEV unsigned comm_off(unsigned o) { return ((o >> 13) << 14) | (o & 8191u); }
DEV unsigned comm_base(unsigned comm, unsigned pol, int s) {
  const unsigned pg = (unsigned)s / SPP, r = (unsigned)s - pg * SPP;
  return comm + pg * 2u * MB + r * NBLK * 16384u + (((pol >> pg) & 1u) << 13);
}
#define TUNEV(i, d) (d)

struct Params {
  const bf16 *x, *wqkv, *wo, *mem0, *Wq, *Wk, *Wv, *Wo, *gw;
  bf16* out;
  unsigned char* ws;
};

DEV rsrc_t make_rsrc(const void* p, unsigned bytes) { return __builtin_amdgcn_make_buffer_rsrc((void*)p, 0, bytes, 0x00020000); }
DEV unsigned comm_start(const unsigned char* ws) {
  const unsigned lo = (unsigned)((unsigned long long)ws & (2u * MB - 1));
  return OFF_COMM + ((2u * MB - lo) & (2u * MB - 1));
}
DEV unsigned xcc_id() { return (unsigned)__builtin_amdgcn_s_getreg(20 | (3 << 11)) & 7u; }
DEV unsigned pk2(float a, float b) { bf16x2 v = {(bf16)a, (bf16)b}; return __builtin_bit_cast(unsigned, v); }
DEV unsigned nosent(unsigned v) { return v < 0xFFFFFFFEu ? v : 0xFFFFFFFEu; }
DEV float rbf(float a) { return (float)(bf16)a; }
DEV bf16x8 as8(u32x4 v) { return __builtin_bit_cast(bf16x8, v); }
DEV bf16x16 cat16(u32x4 a, u32x4 b) { u32x8 v = {a.x, a.y, a.z, a.w, b.x, b.y, b.z, b.w}; return __builtin_bit_cast(bf16x16, v); }
DEV f32x4 mfma16(u32x4 a, u32x4 b, f32x4 c) { return __builtin_amdgcn_mfma_f32_16x16x32_bf16(as8(a), as8(b), c, 0, 0, 0); }
DEV u32x4 ld16(rsrc_t r, unsigned v, unsigned s) { return __builtin_amdgcn_raw_buffer_load_b128(r, v, s, 0); }
DEV u32x4 ld16c(rsrc_t r, unsigned v, unsigned s) { return __builtin_amdgcn_raw_buffer_load_b128(r, v, s, SC1); }
DEV u32x2 ld8c(rsrc_t r, unsigned v, unsigned s) { return __builtin_amdgcn_raw_buffer_load_b64(r, v, s, SC1); }
DEV unsigned ld4c(rsrc_t r, unsigned v, unsigned s) { return __builtin_amdgcn_raw_buffer_load_b32(r, v, s, SC1); }
DEV void st16c(u32x4 d, rsrc_t r, unsigned v, unsigned s) { __builtin_amdgcn_raw_buffer_store_b128(d, r, v, s, SC1); }
DEV void st8c(u32x2 d, rsrc_t r, unsigned v, unsigned s) { __builtin_amdgcn_raw_buffer_store_b64(d, r, v, s, SC1); }
DEV void st4c(unsigned d, rsrc_t r, unsigned v, unsigned s) { __builtin_amdgcn_raw_buffer_store_b32(d, r, v, s, SC1); }
DEV unsigned umax(unsigned a, unsigned b) { return a > b ? a : b; }
DEV unsigned max4(u32x4 v) { return umax(umax(umax(v.x, v.y), v.z), v.w); }
DEV bool any_unwritten(unsigned m, int& budget) {
  const unsigned thr = --budget >= 0 ? SENT - 1u : SENT;
  return __builtin_amdgcn_ballot_w64(m > thr) != 0;
}
#define FENCE() asm volatile("" ::: "memory")
#define BAR_LDS() asm volatile("s_waitcnt lgkmcnt(0)\n\ts_barrier" ::: "memory")
#define BAR_ALL() asm volatile("s_waitcnt vmcnt(0) lgkmcnt(0)\n\ts_barrier" ::: "memory")
#define DRAIN() asm volatile("s_waitcnt vmcnt(0)" ::: "memory")
#define SCHED() __builtin_amdgcn_sched_barrier(0)
DEV float xor32(float v) { return __shfl_xor(v, 32); }
DEV float max2(float a, float b) { return __builtin_amdgcn_fmed3f(a, b, __builtin_inff()); }
DEV float vmax(float a, float b) { float d; asm("v_max_f32 %0, %1, %2" : "=v"(d) : "v"(a), "v"(b)); return d; }
DEV float max_rows(float v) {
  auto a = __builtin_amdgcn_permlane16_swap(__float_as_uint(v), __float_as_uint(v), false, false);
  v = vmax(__uint_as_float(a[0]), __uint_as_float(a[1]));
  auto b = __builtin_amdgcn_permlane32_swap(__float_as_uint(v), __float_as_uint(v), false, false);
  return vmax(__uint_as_float(b[0]), __uint_as_float(b[1]));
}
DEV float sum_rows(float v) {
  auto a = __builtin_amdgcn_permlane16_swap(__float_as_uint(v), __float_as_uint(v), false, false);
  v = __uint_as_float(a[0]) + __uint_as_float(a[1]);
  auto b = __builtin_amdgcn_permlane32_swap(__float_as_uint(v), __float_as_uint(v), false, false);
  return __uint_as_float(b[0]) + __uint_as_float(b[1]);
}
DEV unsigned atomic_inc(unsigned char* ws, int line) {
  return __hip_atomic_fetch_add((unsigned*)(ws + OFF_CTRL + line * 128), 1u, __ATOMIC_RELAXED, __HIP_MEMORY_SCOPE_AGENT);
}
DEV unsigned wait_lines(rsrc_t r, int& budget, int first, int n, unsigned want, int lane, int nap) {
  const unsigned a = OFF_CTRL + (unsigned)(first + (lane < n ? lane : 0)) * 128u;
  unsigned v;
#pragma unroll 1
  for (;;) {
    FENCE();
    v = ld4c(r, a, 0);
    if (__builtin_amdgcn_ballot_w64(v < want) == 0 || --budget < 0) break;
    if (nap) __builtin_amdgcn_s_sleep(8);
  }
  if (n > 1) {
#pragma unroll
    for (int o = 1; o < 64; o <<= 1) { unsigned t = __shfl_xor(v, o); v = t < v ? t : v; }
  }
  FENCE();
  return __builtin_amdgcn_readfirstlane(v);
}
#define FDECL() do {} while (0)
#define FSTAMP(k) do {} while (0)
#define FSAVE(s) do {} while (0)
#define FDUMP() do {} while (0)
#define STAMP(w, role, idx) do {} while (0)
DEV unsigned now() { unsigned long long t; asm volatile("s_memtime %0\n\ts_waitcnt lgkmcnt(0)" : "=s"(t)::"memory"); return (unsigned)t; }
DEV void glds16(const void* g, unsigned lds_dst) {
  unsigned keep;
  asm volatile("s_mov_b32 %0, m0\n\ts_mov_b32 m0, %2\n\ts_nop 0\n\tglobal_load_lds_dwordx4 %1, off\n\ts_mov_b32 m0, %0"
               : "=&s"(keep) : "v"(g), "s"(lds_dst) : "memory");
}
DEV unsigned lds_addr(const void* p) { return (unsigned)(unsigned long long)(const __attribute__((address_space(3))) void*)p; }

DEV u32x4 gather8(const bf16* p, int stride) {
  const unsigned short* q = (const unsigned short*)p;
  unsigned v[8];
#pragma unroll
  for (int j = 0; j < 8; j++) v[j] = q[(size_t)j * stride];
  return (u32x4){v[0] | (v[1] << 16), v[2] | (v[3] << 16), v[4] | (v[5] << 16), v[6] | (v[7] << 16)};
}
DEV int xcol(int cg, int c) { return 16 * (cg >> 1) + 4 * (c >> 1) + 2 * (cg & 1) + (c & 1); }

__global__ void __launch_bounds__(512) prep_kernel(Params p) {
  __shared__ unsigned best[16];
  const int tid = threadIdx.x, lane = tid & 63, wave = __builtin_amdgcn_readfirstlane(tid >> 6);
  const int gt = blockIdx.x * 512 + tid;
  constexpr int nthr = 256 * 512;
  unsigned char* ws = p.ws;
  const unsigned comm = comm_start(ws);
  if (blockIdx.x < NPAGE) {
    const rsrc_t r = make_rsrc(ws, WS_USED);
    const unsigned base = comm + blockIdx.x * 2u * MB + 126u * 16384u + wave * 1024u;
    if (lane < 8) st4c(1u, r, base + (lane & 1) * 8192u + (lane >> 1) * 256u, 0);
    DRAIN();
    unsigned b0 = 0xFFFFFFFFu, b1 = 0xFFFFFFFFu, acc = 0;
#pragma unroll
    for (int i = 0; i < 6; i++) {
      const unsigned t0 = now();
      acc += ld4c(r, base + (i & 1) * 8192u + (i >> 1) * 256u, 0);
      asm volatile("" : "+v"(acc));
      const unsigned d = now() - t0;
      if (i & 1) b1 = d < b1 ? d : b1; else b0 = d < b0 ? d : b0;
    }
    if (lane == 0) { best[wave] = b0; best[8 + wave] = b1; }
    __syncthreads();
    if (tid == 0) {
      for (int i = 1; i < 8; i++) { b0 = best[i] < b0 ? best[i] : b0; b1 = best[8 + i] < b1 ? best[8 + i] : b1; }
      unsigned pol = (b1 < b0 ? 1u : 0u) ^ (xcc_id() >> 2);
      *(unsigned*)(ws + OFF_CTRL + (C_POL + blockIdx.x) * 128) = pol;
    }
  }
  if (gt >= nthr - 40 * 20 * 64) {
    const int u = gt - (nthr - 40 * 20 * 64), l = u & 63, f = u >> 6, ks = f % 20, tile = f / 20;
    *(u32x4*)(ws + OFF_WO + (size_t)u * 16) = gather8(p.wo + (size_t)(ks * 32 + (l >> 4) * 8) * DM + tile * 16 + (l & 15), DM);
  }
  {
    const int row = blockIdx.x * 8 + wave;
    float s = 0;
    unsigned char* dst = ws + OFF_XF + (size_t)(row >> 4) * 20480 + (row & 15) * 16;
#pragma unroll
    for (int i = 0; i < 2; i++) {
      const int ch = lane + 64 * i;
      if (ch < 80) {
        const u32x4 xv = *(const u32x4*)(p.x + (size_t)row * DM + ch * 8), gv = *(const u32x4*)(p.gw + ch * 8);
        *(u32x4*)(dst + ch * 256) = xv;
        const bf16x8 a = as8(xv), b = as8(gv);
#pragma unroll
        for (int j = 0; j < 8; j++) s += rbf((float)a[j] * (float)b[j]);
      }
    }
#pragma unroll
    for (int o = 1; o < 64; o <<= 1) s += __shfl_xor(s, o);
    s = rbf(s);
    if (lane == 0) *(float*)(ws + OFF_GATE + row * 4) = rbf(1.f / (1.f + __expf(-s)));
  }
  if (gt < C_LINES && (gt < C_POL || gt >= C_POL + NPAGE)) *(unsigned*)(ws + OFF_CTRL + gt * 128) = 0;
  if (gt < NH * 64 * 4)
    *(u32x4*)(ws + OFF_KV + (size_t)(gt >> 2) * KVT + 1280 + (gt & 3) * 384 + 20 * 16) = (u32x4){0x3F803F80u, 0x3F803F80u, 0x3F803F80u, 0x3F803F80u};
  for (int u = gt; u < NSEG * NBLK * 1024; u += nthr) {
    const int slot = u >> 10, st = slot / NBLK, blk = slot - st * NBLK, pg = st / SPP, w16 = u & 511;
    u32x4 val = {SENT, SENT, SENT, SENT};
    const unsigned lo = ((unsigned)blk & 3u) * 8192u + (unsigned)w16 * 16u;
    if (st == 0 && lo < 10240u) {
      const int q = lo >> 4, cg = q >> 3, i = q & 7;
      const unsigned* m = (const unsigned*)(p.mem0 + i * DM + xcol(cg, 0));
      val = (u32x4){nosent(m[0]), nosent(m[2]), nosent(m[4]), nosent(m[6])};
    }
    if (lo < (blk < 8 ? 30720u : 10240u))
      *(u32x4*)(ws + comm + (unsigned)pg * 2u * MB + (unsigned)(slot - pg * SPP * NBLK) * 16384u + (unsigned)(u & 1023) * 16u) = val;
  }
}

struct Ctx {
  const Params& p;
  rsrc_t r;
  unsigned char* lds;
  int lane, wave, role;
  mutable int budget;
};

DEV void pin_w40(u32x4 (&w)[40]) {
#pragma unroll
  for (int i = 0; i < 40; i++) asm volatile("" : "+v"(w[i]));
}
constexpr unsigned XB0 = 65536, XB1 = 0;
DEV void stage_x(const Ctx& c, int seg, int buf) {
  const unsigned char* src = c.p.ws + OFF_XF + (size_t)seg * 40960 + c.wave * 5120 + c.lane * 16;
  const unsigned dst = lds_addr(c.lds) + (buf ? XB1 : XB0) + c.wave * 5120;
#pragma unroll
  for (int i = 0; i < 5; i++) glds16(src + i * 1024, dst + i * 1024);
}
DEV void dma_block(const Ctx& c, const bf16* src, int ld, unsigned lds_off) {
  const int q = c.lane >> 4;
#pragma unroll
  for (int i = 0; i < 4; i++)
    glds16(src + (size_t)(c.wave * 16 + i * 4 + q) * ld + (((c.lane & 15) ^ ((q << 2) | i)) << 3), lds_addr(c.lds) + lds_off + (c.wave * 16 + i * 4) * 256);
}
typedef __attribute__((address_space(3))) s16x4* lds_s16x4;
DEV void read_cols(const Ctx& c, unsigned lds_off, int col0, int cstep, u32x4* f) {
  const int g = c.lane >> 4, q = (c.lane >> 2) & 3, col = col0 + cstep * (c.lane & 3);
  const unsigned a0 = lds_addr(c.lds) + lds_off + (8 * g + q) * 256 + (((col >> 3) ^ ((q << 2) | ((2 * g) & 3))) << 4) + ((col >> 2) & 1) * 8;
  const unsigned a1 = lds_addr(c.lds) + lds_off + (8 * g + 4 + q) * 256 + (((col >> 3) ^ ((q << 2) | ((2 * g + 1) & 3))) << 4) + ((col >> 2) & 1) * 8;
#pragma unroll
  for (int ks = 0; ks < 4; ks++) {
    const u32x2 lo = __builtin_bit_cast(u32x2, __builtin_amdgcn_ds_read_tr16_b64_v4i16((lds_s16x4)(size_t)(a0 + ks * 8192)));
    const u32x2 hi = __builtin_bit_cast(u32x2, __builtin_amdgcn_ds_read_tr16_b64_v4i16((lds_s16x4)(size_t)(a1 + ks * 8192)));
    f[ks] = (u32x4){lo.x, lo.y, hi.x, hi.y};
  }
}
#define WAIT_BAR(n) asm volatile("s_waitcnt vmcnt(%0) lgkmcnt(0)\n\ts_barrier" ::"n"(n) : "memory")
template <int MODE>
DEV void load_weights(const Ctx& c, u32x4 (&w)[40], const bf16* A, int lda, int stepa, const bf16* B, int ldb, int stepb, int xseg) {
#pragma unroll
  for (int j = 0; j < 2; j++) { dma_block(c, A + (size_t)j * stepa, lda, j * 65536); dma_block(c, B + (size_t)j * stepb, ldb, j * 65536 + 32768); }
#pragma unroll
  for (int j = 0; j < 5; j++) {
    if (j < 4) WAIT_BAR(8); else if (xseg >= 0) WAIT_BAR(5); else WAIT_BAR(0);
    const unsigned o = (j & 1) * 65536;
    if (MODE == 2) {
      read_cols(c, o, 16 * c.wave, 4, &w[4 * j]);
      read_cols(c, o + 32768, 16 * c.wave, 4, &w[20 + 4 * j]);
    } else {
      read_cols(c, o + (c.wave >> 2) * 32768, 32 * (c.wave & 3), MODE ? 4 : 8, &w[4 * j]);
      read_cols(c, o + (c.wave >> 2) * 32768, 32 * (c.wave & 3) + (MODE ? 16 : 4), MODE ? 4 : 8, &w[20 + 4 * j]);
    }
    BAR_LDS();
    if (j + 2 < 5) { dma_block(c, A + (size_t)(j + 2) * stepa, lda, o); dma_block(c, B + (size_t)(j + 2) * stepb, ldb, o + 32768); }
    if (j == 3 && xseg >= 0) stage_x(c, xseg, 0);
  }
  pin_w40(w);
}
DEV void make_vo(const Ctx& c, int id) {
  const int lane = c.lane, wave = c.wave, n = lane & 15, g = lane >> 4, h = id >> 2, q0 = (id & 3) * 5;
  const bf16* B = c.p.Wo + (size_t)h * 128 * DM;
  u32x4 w[20];
#pragma unroll
  for (int j = 0; j < 3; j++) dma_block(c, B + j * 128, DM, j * 32768);
#pragma unroll
  for (int i = 0; i < 5; i++)
    glds16(c.p.Wv + (size_t)((q0 + i) * 32 + 8 * (n >> 2) + 4 * (wave >> 2) + (n & 3)) * 4096 + h * 128 + (wave & 3) * 32 + g * 8, lds_addr(c.lds) + 98304 + (i * 8 + wave) * 1024);
  WAIT_BAR(9);
  read_cols(c, 0, 16 * wave, 4, &w[0]);
  read_cols(c, 32768, 16 * wave, 4, &w[4]);
  BAR_LDS();
  dma_block(c, B + 384, DM, 0);
  dma_block(c, B + 512, DM, 32768);
  WAIT_BAR(8);
  read_cols(c, 65536, 16 * wave, 4, &w[8]);
  WAIT_BAR(0);
  read_cols(c, 0, 16 * wave, 4, &w[12]);
  read_cols(c, 32768, 16 * wave, 4, &w[16]);
  const unsigned lo = (unsigned)(2 * wave + ((n >> 1) & 1)) * (320u * KB) + (unsigned)((h & 1) * 8 + 2 * (n >> 2) + (n & 1) + 16 * g) * 16u;
#pragma unroll 1
  for (int i = 0; i < 5; i++) {
    u32x4 va[8];
#pragma unroll
    for (int k = 0; k < 8; k++) va[k] = *(const u32x4*)(c.lds + 98304 + (i * 8 + k) * 1024 + lane * 16);
    SCHED();
#pragma unroll
    for (int t = 0; t < 5; t++) {
      f32x4 z0 = {0, 0, 0, 0}, z1 = {0, 0, 0, 0};
#pragma unroll
      for (int ke = 0; ke < 4; ke++) {
        z0 = mfma16(va[ke], w[t * 4 + ke], z0);
        z1 = mfma16(va[4 + ke], w[t * 4 + ke], z1);
      }
      st16c((u32x4){pk2(z0[0], z0[1]), pk2(z0[2], z0[3]), pk2(z1[0], z1[1]), pk2(z1[2], z1[3])}, c.r, lo,
            OFF_VO + (unsigned)t * 16u * (320u * KB) + (unsigned)((h >> 1) * 20 + q0 + i) * KB);
    }
  }
  BAR_ALL();
  if (wave == 0 && lane == 0) atomic_inc(c.p.ws, C_VO + (id >> 5));
}

DEV void gather2(const Ctx& c, unsigned base, unsigned base2, unsigned a0, unsigned char* dst) {
  u32x4 v0, v1;
  bool again;
#pragma unroll 1
  do {
    FENCE();
    v0 = ld16c(c.r, a0, base); v1 = ld16c(c.r, a0 + 1024u, base);
    base = base2;
    again = any_unwritten(umax(max4(v0), max4(v1)), c.budget);
  } while (again);
  *(u32x4*)dst = v0;
  *(u32x4*)(dst + 1024) = v1;
}
DEV void wait_until(unsigned t) {
#pragma unroll 4
  for (int i = 0; i < 256; i++) if ((int)(now() - t) >= 0) break;
}
struct Prog {
  unsigned v, addr;
  DEV void init(int first, int n, int lane) { v = 0; addr = OFF_CTRL + (unsigned)(first + (lane >> 5) * 32 + (n > 1 ? lane & 31 : 0)) * 128u; }
  DEV bool lacks(int s, int lane) const { return __builtin_amdgcn_ballot_w64((lane >> 5) == (s & 1) && v < (unsigned)(s >> 1) + 1u) != 0; }
  DEV void need(const Ctx& c, int s) {
    if (lacks(s, c.lane)) {
      bool again;
#pragma unroll 1
      do {
        FENCE();
        v = ld4c(c.r, addr, 0);
        asm volatile("" : "+v"(v));
        const bool more = --c.budget >= 0;
        again = lacks(s, c.lane) & more;
      } while (again);
    }
    FENCE();
  }
  DEV void refresh(const Ctx& c, int s) { if (s < NSEG && lacks(s, c.lane)) v = ld4c(c.r, addr, 0); }
};
DEV void mem_times_w(const u32x4 (&w)[40], const unsigned char* src, int idx, f32x4& d0, f32x4& d1) {
  u32x4 a[10];
#pragma unroll
  for (int ks = 0; ks < 10; ks++) a[ks] = *(const u32x4*)(src + ks * 1024);
  d0 = (f32x4){0, 0, 0, 0}; d1 = (f32x4){0, 0, 0, 0};
  SCHED();
#pragma unroll
  for (int ks = 0; ks < 10; ks++) {
    d0 = __builtin_amdgcn_smfmac_f32_16x16x64_bf16(as8(a[ks]), cat16(w[2 * ks], w[2 * ks + 1]), d0, idx, 0, 0);
    d1 = __builtin_amdgcn_smfmac_f32_16x16x64_bf16(as8(a[ks]), cat16(w[20 + 2 * ks], w[21 + 2 * ks]), d1, idx, 0, 0);
  }
  SCHED();
}

DEV void softmax12(const f32x4& s0, const f32x4& s1, const f32x4& sm, u32x4& pt, u32x2& pm) {
  const float cs = 0.08838834764831845f * 1.4426950408889634f;
  float v[12];
#pragma unroll
  for (int j = 0; j < 4; j++) { v[j] = s0[j]; v[4 + j] = s1[j]; v[8 + j] = sm[j]; }
  float mx = max2(v[8], v[9]);
  mx = __builtin_fmaxf(__builtin_fmaxf(mx, v[10]), v[11]);
#pragma unroll
  for (int j = 0; j < 8; j++) mx = __builtin_fmaxf(mx, v[j]);
  const float mc = -max_rows(mx) * cs;
#pragma unroll
  for (int j = 0; j < 12; j++) v[j] = __builtin_amdgcn_exp2f(__builtin_fmaf(v[j], cs, mc));
  const float sum = sum_rows(((v[0] + v[1]) + (v[2] + v[3])) + ((v[4] + v[5]) + (v[6] + v[7])) + ((v[8] + v[9]) + (v[10] + v[11])));
  const float inv = __builtin_amdgcn_rcpf(sum);
  pt = (u32x4){nosent(pk2(v[0] * inv, v[1] * inv)), nosent(pk2(v[2] * inv, v[3] * inv)), nosent(pk2(v[4] * inv, v[5] * inv)), nosent(pk2(v[6] * inv, v[7] * inv))};
  pm = (u32x2){nosent(pk2(v[8] * inv, v[9] * inv)), nosent(pk2(v[10] * inv, v[11] * inv))};
}
DEV float dpp_ror8(float v) { return __uint_as_float(__builtin_amdgcn_update_dpp(0u, __float_as_uint(v), 0x128, 0xf, 0xf, true)); }
template <int CLS>
DEV void role_S_loop(const Ctx& c, const u32x4 (&w)[40], int h, unsigned comm, unsigned pol) {
  const int lane = c.lane, wave = c.wave, n = lane & 15, g = lane >> 4;
  const rsrc_t r = c.r;
  Prog pr; pr.init(C_PQK + h, 1, lane);
  const int delay = TUNEV(1, 1700);
  const int idx = (lane & 1) ? 0xEEEE : 0x4444;
  const unsigned ga0 = comm_off(CM_MS + wave * 2048u + lane * 16u);
  const unsigned so0 = CLS == 0 ? comm_off(CM_P + h * 640u + (g * 8 + (n & 7)) * 16u + (n >> 3) * 8u) : lane * 16u;
  const unsigned so1 = CLS == 0 ? comm_off(CM_P + h * 640u + 512u + (n & 7) * 16u + (g & 1) * 8u + (g >> 1) * 4u) : 1024u + n * 16u + (g - 2) * 8u;
  constexpr int NT = CLS == 3 ? 5 : 6;
  const unsigned to = (wave - 5) * 5120u + lane * 16u;
  unsigned char* const xd = c.lds + 20480 + (wave & 3) * 1024 + ((wave >> 2) * 8 + 2 * g + 16 * (n >> 2)) * 16 + (n & 3) * 4;
  const unsigned char* const xq = c.lds + 20480 + ((n & 7) + 16 * g) * 16;
  u32x4 t[NT];
  if (CLS >= 3) {
    pr.need(c, 0);
#pragma unroll
    for (int i = 0; i < NT; i++) t[i] = ld16c(r, to + i * 1024u, OFF_QKT + (unsigned)h * 16u * KB);
#pragma unroll
    for (int i = 0; i < NT; i++) *(u32x4*)(c.lds + 28672 + to + i * 1024) = t[i];
  }
  if (CLS < 2) __builtin_amdgcn_s_setprio(2);
  unsigned tpub = now() - delay;
  for (int s = 0; s < NSEG; s++) {
    FDECL();
    unsigned base = comm_base(comm, pol, s);
    unsigned o_tl = 28672 + (s & 1) * 16384 + lane * 16 + (CLS < 2 ? 8192 + wave * 1024 : 0), o_gd = (s & 1) * 10240 + wave * 2048 + lane * 16;
    unsigned o_as = (s & 1) * 10240 + (2 * g + (lane & 1)) * 128 + (n >> 1) * 16;
    asm volatile("" : "+s"(base), "+v"(o_as));
    if (CLS < 3) asm volatile("" : "+v"(o_gd));
    if (CLS < 2) asm volatile("" : "+v"(o_tl));
    const unsigned char* tl = c.lds + o_tl;
    if (CLS == 0) wait_until(tpub + delay);
    FSTAMP(2);
    BAR_LDS();
    if (CLS < 3) gather2(c, base, base + CM_REP, ga0, c.lds + o_gd);
    if (CLS >= 3 && s + 1 < NSEG) pr.need(c, s + 1);
    FSTAMP(0);
    BAR_LDS();
    if (CLS >= 3 && s + 1 < NSEG) {
#pragma unroll
      for (int i = 0; i < NT; i++) t[i] = ld16c(r, to + i * 1024u, OFF_QKT + (unsigned)(ABL_S(s + 1) * NH + h) * 16u * KB);
      pr.refresh(c, s + 2);
    }
    f32x4 d0, d1;
    mem_times_w(w, c.lds + o_as, idx, d0, d1);
    u32x4 k0, k1;
    if (CLS < 2) { k0 = *(const u32x4*)tl; k1 = *(const u32x4*)(tl + 4096); }
    *(unsigned*)xd = pk2(d0[0] + d0[1], d1[0] + d1[1]);
    *(unsigned*)(xd + 16) = pk2(d0[2] + d0[3], d1[2] + d1[3]);
    if (CLS < 2) {
      FENCE();
      const u32x4 qf = *(const u32x4*)(xq + wave * 1024);
      const f32x4 s0 = mfma16(k0, qf, (f32x4){0, 0, 0, 0}), s1 = mfma16(k1, qf, (f32x4){0, 0, 0, 0});
      *(f32x4*)(c.lds + 24576 + wave * 1024 + lane * 16) = n < 8 ? s0 : s1;
    }
    FSTAMP(1);
    BAR_LDS();
    u32x4 pt; u32x2 pm;
    if (CLS == 0) {
      const float cs = 0.08838834764831845f * 1.4426950408889634f;
      u32x4 kf[4], qf[4]; f32x4 pp[4];
#pragma unroll
      for (int ke = 0; ke < 4; ke++) { kf[ke] = *(const u32x4*)(xq + 128 + ke * 1024); qf[ke] = *(const u32x4*)(xq + ke * 1024); }
#pragma unroll
      for (int ke = 0; ke < 4; ke++) pp[ke] = *(const f32x4*)(c.lds + 24576 + ke * 1024 + lane * 16);
      SCHED();
      f32x4 sm = mfma16(kf[0], qf[0], (f32x4){0, 0, 0, 0});
#pragma unroll
      for (int ke = 1; ke < 4; ke++) sm = mfma16(kf[ke], qf[ke], sm);
      SCHED();
      const f32x4 st = (pp[0] + pp[1]) + (pp[2] + pp[3]);
      float mx = max2(max2(st[0], st[1]), max2(st[2], st[3]));
      const float ma = n < 8 ? sm[0] : sm[1], mb = n < 8 ? sm[2] : sm[3], mv = g < 2 ? ma : mb;
      mx = max2(mx, mv);
      mx = vmax(mx, dpp_ror8(mx));
      const float mc = -max_rows(mx) * cs;
      const float e0 = __builtin_amdgcn_exp2f(__builtin_fmaf(st[0], cs, mc)), e1 = __builtin_amdgcn_exp2f(__builtin_fmaf(st[1], cs, mc));
      const float e2 = __builtin_amdgcn_exp2f(__builtin_fmaf(st[2], cs, mc)), e3 = __builtin_amdgcn_exp2f(__builtin_fmaf(st[3], cs, mc));
      const float e4 = __builtin_amdgcn_exp2f(__builtin_fmaf(mv, cs, mc));
      const float e5 = dpp_ror8(e4);
      float sum = ((e0 + e1) + (e2 + e3)) + e4;
      sum += dpp_ror8(sum);
      const float inv = __builtin_amdgcn_rcpf(sum_rows(sum));
      u32x2 o0 = {nosent(pk2(e0 * inv, e1 * inv)), nosent(pk2(e2 * inv, e3 * inv))};
      unsigned o1 = nosent(pk2(e4 * inv, e5 * inv));
      asm volatile("" : "+v"(o0), "+v"(o1));
      st8c(o0, r, so0, base);
      if (n < 8) st4c(o1, r, so1, base);
      FSTAMP(3);
      st8c(o0, r, so0, base + CM_REP);
      if (n < 8) st4c(o1, r, so1, base + CM_REP);
      tpub = now();
    } else if (CLS == 3) {
      int rel = (wave - 5) * 16 + n - 4 * g;
      asm volatile("" : "+v"(rel));
      const float mneg = g >= 2 ? 0.f : -3.0e38f;
      f32x4 sm = {mneg, mneg, mneg, mneg}, s0, s1;
#pragma unroll
      for (int j = 0; j < 4; j++) { s0[j] = j <= rel ? 0.f : -3.0e38f; s1[j] = 16 + j <= rel ? 0.f : -3.0e38f; }
#pragma unroll
      for (int ke = 0; ke < 4; ke++) {
        const u32x4 qt = *(const u32x4*)(tl + (wave - 5) * 4096 + ke * 1024);
        s0 = mfma16(*(const u32x4*)(tl + 8192 + ke * 1024), qt, s0);
        s1 = mfma16(*(const u32x4*)(tl + 12288 + ke * 1024), qt, s1);
        sm = mfma16(*(const u32x4*)(c.lds + 20480 + ke * 1024 + lane * 16), qt, sm);
      }
      softmax12(s0, s1, sm, pt, pm);
      DRAIN();
    }
    BAR_LDS();
    if (CLS >= 3) {
      if (CLS == 4 && lane == 0) st4c(s, r, OFF_CTRL + (C_PCH + h) * 128, 0);
#pragma unroll
      for (int i = 0; i < NT; i++) *(u32x4*)(c.lds + 28672 + ((s + 1) & 1) * 16384 + to + i * 1024) = t[i];
      if (CLS == 3 && ABL_PT(1)) {
        const unsigned sb = OFF_PT + (unsigned)((s * NH + h) * 2 + wave - 5) * PTT;
        st16c(pt, r, so0, sb);
        if (g >= 2) st8c(pm, r, so1, sb);
      }
    }
    FSAVE(s);
  }
}
DEV void role_S(const Ctx& c, int h, unsigned comm, unsigned pol) {
  u32x4 w[40];
  load_weights<0>(c, w, c.p.Wq + h * 128, 4096, 128 * 4096, c.p.Wk + h * 128, 4096, 128 * 4096, -1);
  STAMP(5, c.role, 6);
  if (c.wave == 0) role_S_loop<0>(c, w, h, comm, pol);
  else if (c.wave < 4) role_S_loop<1>(c, w, h, comm, pol);
  else if (c.wave == 4) role_S_loop<2>(c, w, h, comm, pol);
  else if (c.wave < 7) role_S_loop<3>(c, w, h, comm, pol);
  else role_S_loop<4>(c, w, h, comm, pol);
  BAR_ALL();
  if (c.wave == 7 && c.lane == 0) st4c(NSEG, c.r, OFF_CTRL + (C_PCH + h) * 128, 0);
  FDUMP();
}

template <int CLS>
DEV void role_X_loop(const Ctx& c, const u32x4 (&w)[40], int xc, unsigned comm, unsigned pol) {
  const int lane = c.lane, wave = c.wave, n = lane & 15, g = lane >> 4;
  const rsrc_t r = c.r;
  Prog pr; pr.init(C_PVZ, 32, lane);
  const int delay = TUNEV(2, 2950), delay_m = TUNEV(3, 0);
  const int idx = (lane & 1) ? 0xEEEE : 0x4444;
  const unsigned ga0 = comm_off(CM_MX + (wave - 3) * 2048u + lane * 16u);
  const int h0 = 4 * wave + (n >> 3);
  const unsigned pa0 = comm_off(CM_P + h0 * 640u + (g * 8 + (n & 7)) * 16u), pa1 = comm_off(CM_P + (h0 + 2) * 640u + (g * 8 + (n & 7)) * 16u);
  const unsigned pb = comm_off(CM_P + (h0 + 2 * (g >> 1)) * 640u + 512u + (n & 7) * 16u + (g & 1) * 8u);
  const unsigned mo = comm_off(CM_MS + xc * 128u + n * 8u + (g & 1) * 4u);
  float* const rw = (float*)(c.lds + 10240 + (wave * 2 + (g >> 1)) * 256 + ((n & 7) * 2 + (g & 1)) * 16);
  const float* const rr = (const float*)(c.lds + 10240 + g * 1024 + n * 16);
  constexpr int NT = CLS == 0 ? 4 : 6;
  const int t0 = CLS == 0 ? 0 : 6 * wave - 2;
  const unsigned tg = (unsigned)((2 * t0 + (lane >> 5)) * 80 + xc) * 512u + (lane & 31) * 16u;
  u32x4 t[NT];
  if (CLS < 2) {
    pr.need(c, 0);
#pragma unroll
    for (int i = 0; i < NT; i++) t[i] = ld16c(r, tg + i * 81920u, OFF_ZT);
#pragma unroll
    for (int i = 0; i < NT; i++) *(u32x4*)(c.lds + 18432 + (t0 + i) * 1024 + lane * 16) = t[i];
  }
  unsigned tpub = now() - delay;
  for (int s = 0; s < NSEG; s++) {
    FDECL();
    unsigned base = comm_base(comm, pol, s), nbase = comm_base(comm, pol, s + 1);
    if (CLS == 2) { wait_until(tpub + delay_m); gather2(c, base, base, ga0, c.lds + (wave - 3) * 2048 + lane * 16); }
    else if (s + 2 < NSEG) pr.need(c, s + 1);
    FSTAMP(0);
    BAR_LDS();
    if (CLS < 2 && s + 2 < NSEG) {
#pragma unroll
      for (int i = 0; i < NT; i++) t[i] = ld16c(r, tg + i * 81920u, OFF_ZT + (unsigned)ABL_S(s + 1) * (NH * 80u * 512u));
      pr.refresh(c, s + 2);
    }
    f32x4 d0, d1;
    mem_times_w(w, c.lds + (2 * g + (lane & 1)) * 128 + (n >> 1) * 16, idx, d0, d1);
    const unsigned char* zl = c.lds + 18432 + (s & 1) * 16384 + h0 * 512 + (g * 8 + (n & 7)) * 16;
    u32x4 zt0 = *(const u32x4*)zl, zt1 = *(const u32x4*)(zl + 1024);
    const auto za = __builtin_amdgcn_permlane32_swap(pk2(d0[0] + d0[1], d0[2] + d0[3]), pk2(d1[0] + d1[1], d1[2] + d1[3]), false, false);
    const auto zb = __builtin_amdgcn_permlane16_swap(za[0], za[1], false, false);
    u32x2 zm = {zb[0], zb[1]};
    *(u32x2*)(c.lds + 14336 + (h0 + 2 * (g >> 1)) * 128 + (n & 7) * 16 + (g & 1) * 8) = zm;
    if (s + 1 < NSEG) {
      unsigned base2 = base + CM_REP;
      asm volatile("" : "+s"(nbase), "+s"(base2), "+v"(zm), "+v"(zt0), "+v"(zt1));
      if (CLS == 0) wait_until(tpub + delay);
      FSTAMP(1);
      BAR_LDS();
      u32x4 p0, p1; u32x2 pm;
      bool again;
#pragma unroll 1
      do {
        FENCE();
        p0 = ld16c(r, pa0, base); p1 = ld16c(r, pa1, base); pm = ld8c(r, pb, base);
        base = base2;
        again = any_unwritten(umax(umax(max4(p0), max4(p1)), umax(pm.x, pm.y)), c.budget);
      } while (again);
      DRAIN();
      f32x4 acc = mfma16(zt0, p0, (f32x4){0, 0, 0, 0});
      f32x4 acc1 = __builtin_amdgcn_mfma_f32_16x16x16bf16_1k(__builtin_bit_cast(s16x4, zm), __builtin_bit_cast(s16x4, pm), (f32x4){0, 0, 0, 0}, 0, 0, 0);
      acc = mfma16(zt1, p1, acc);
      acc += acc1;
      if ((n >> 3) == (g >> 1)) *(f32x4*)rw = acc;
      FSTAMP(2);
      BAR_LDS();
      if (CLS == 0) {
        const f32x4 t0_ = *(const f32x4*)rr, t1_ = *(const f32x4*)(rr + 64), t2_ = *(const f32x4*)(rr + 128), t3_ = *(const f32x4*)(rr + 192);
        const f32x4 tt = (t0_ + t1_) + (t2_ + t3_);
        const auto a = __builtin_amdgcn_permlane16_swap(__float_as_uint(tt[0]), __float_as_uint(tt[2]), false, false);
        const auto b = __builtin_amdgcn_permlane16_swap(__float_as_uint(tt[1]), __float_as_uint(tt[3]), false, false);
        const float u0 = __uint_as_float(a[0]) + __uint_as_float(a[1]), u1 = __uint_as_float(b[0]) + __uint_as_float(b[1]);
        const auto e = __builtin_amdgcn_permlane32_swap(__float_as_uint(u0), __float_as_uint(u0), false, false);
        const auto f = __builtin_amdgcn_permlane32_swap(__float_as_uint(u1), __float_as_uint(u1), false, false);
        const unsigned o = nosent(pk2(__uint_as_float(e[0]) + __uint_as_float(e[1]), __uint_as_float(f[0]) + __uint_as_float(f[1])));
        st4c(o, r, mo + (g >> 1) * 2u * CM_REP, nbase);
        FSTAMP(3);
        if (g < 2) st4c(o, r, mo + CM_REP, nbase);
        tpub = now();
      }
    }
    BAR_LDS();
    if (CLS == 2) tpub = now();
    if (CLS < 2) {
      if (CLS == 0 && lane == 0 && s + 1 < NSEG) st4c(s, r, OFF_CTRL + (C_PCH + 32 + xc) * 128, 0);
      if (CLS == 1) {
#pragma unroll
        for (int i = 0; i < 2; i++) {
          const int hb = (wave - 1) * 16 + i * 8;
          st16c(*(const u32x4*)(c.lds + 14336 + hb * 128 + lane * 16), r, (unsigned)((hb + (lane >> 3)) * 80 + xc) * 128u + (lane & 7) * 16u, OFF_ZM + (unsigned)s * (NH * 80u * 128u));
        }
      }
#pragma unroll
      for (int i = 0; i < NT; i++) *(u32x4*)(c.lds + 18432 + ((s + 1) & 1) * 16384 + (t0 + i) * 1024 + lane * 16) = t[i];
    }
    FSAVE(s);
  }
}
DEV void role_X(const Ctx& c, int xc, unsigned comm, unsigned pol) {
  u32x4 w[40];
  if (c.wave == 0) wait_lines(c.r, c.budget, C_VO, 4, 32, c.lane, 0);
  BAR_LDS();
  STAMP(5, c.role, 7);
#pragma unroll
  for (int i = 0; i < 40; i++) w[i] = ld16c(c.r, c.lane * 16u + (i & 3) * 1024u, OFF_VO + (unsigned)(xc * 8 + c.wave) * 40u * KB + (i >> 2) * 4096u);
  pin_w40(w);
  STAMP(5, c.role, 6);
  if (c.wave == 0) role_X_loop<0>(c, w, xc, comm, pol);
  else if (c.wave < 3) role_X_loop<1>(c, w, xc, comm, pol);
  else role_X_loop<2>(c, w, xc, comm, pol);
  BAR_ALL();
  if (c.wave == 0 && c.lane == 0) st4c(NSEG, c.r, OFF_CTRL + (C_PCH + 32 + xc) * 128, 0);
  FDUMP();
}

template <int N, int D, class A, class F>
DEV void piped(A addr, F use) {
  u32x4 b[D];
#pragma unroll
  for (int i = 0; i < D; i++) b[i] = *(const u32x4*)addr(i);
  SCHED();
#pragma unroll
  for (int i = 0; i < N; i++) {
    use(i, b[i % D]);
    if (i + D < N) b[i % D] = *(const u32x4*)addr(i + D);
    SCHED();
  }
}
template <int NST>
DEV void seg_end(const Ctx& c, int s, int line) {
  if (s < 2 || s == NSEG / 2 - 1) {
    BAR_ALL();
    if (c.wave == 0 && c.lane == 0) st4c(s + 1, c.r, OFF_CTRL + line * 128, 0);
  } else {
    asm volatile("s_waitcnt vmcnt(%0) lgkmcnt(0)\n\ts_barrier" ::"n"(NST) : "memory");
    if (c.wave == 0 && c.lane == 0) st4c(s, c.r, OFF_CTRL + line * 128, 0);
  }
}

DEV void role_QK(const Ctx& c, int h, int par) {
  const int lane = c.lane, wave = c.wave;
  const rsrc_t r = c.r;
  u32x4 w[40];
  load_weights<0>(c, w, c.p.Wq + h * 128, 4096, 128 * 4096, c.p.Wk + h * 128, 4096, 128 * 4096, par);
  STAMP(5, c.role, 6);
  BAR_ALL();
  for (int j = 0; j < NSEG / 2; j++) {
    const int s = 2 * j + par;
    if (s + 2 < NSEG) stage_x(c, s + 2, (j + 1) & 1);
    const unsigned char* xb = c.lds + ((j & 1) ? XB1 : XB0) + lane * 16;
    const unsigned b = OFF_QKT + (unsigned)(s * NH + h) * 16u * KB + (wave >> 2) * 8192u + (wave & 3) * 1024u;
    f32x4 d[2][2] = {};
    piped<40, 8>([&](int i) { return xb + i * 1024; }, [&](int i, const u32x4& xf) {
      d[i / 20][0] = mfma16(w[i % 20], xf, d[i / 20][0]);
      d[i / 20][1] = mfma16(w[20 + i % 20], xf, d[i / 20][1]);
    });
#pragma unroll
    for (int rt = 0; rt < 2; rt++)
      st16c((u32x4){pk2(d[rt][0][0], d[rt][1][0]), pk2(d[rt][0][1], d[rt][1][1]), pk2(d[rt][0][2], d[rt][1][2]), pk2(d[rt][0][3], d[rt][1][3])}, r, lane * 16u, b + rt * 4096u);
    seg_end<2>(c, j, C_PQK + par * 32 + h);
    STAMP(5, c.role, 16 + j);
  }
}

DEV void role_VZ(const Ctx& c, int h, int par) {
  const int lane = c.lane, wave = c.wave, n = lane & 15, g = lane >> 4;
  const rsrc_t r = c.r;
  u32x4 w[40];
  load_weights<2>(c, w, c.p.Wv + h * 128, 4096, 128 * 4096, c.p.Wo + (size_t)h * 128 * DM, DM, 128, par);
  STAMP(5, c.role, 6);
  BAR_ALL();
  unsigned char* vf = c.lds + 49152;
  for (int j = 0; j < NSEG / 2; j++) {
    const int s = 2 * j + par;
    FDECL();
    FSTAMP(3);
    if (s + 2 < NSEG) stage_x(c, s + 2, (j + 1) & 1);
    const unsigned char* xb = c.lds + ((j & 1) ? XB1 : XB0) + lane * 16;
    f32x4 d0 = {0, 0, 0, 0}, d1 = {0, 0, 0, 0};
    piped<40, 12>([&](int i) { return xb + ((i & 1) * 20 + (i >> 1)) * 1024; }, [&](int i, const u32x4& xf) {
      if (i & 1) d1 = mfma16(w[i >> 1], xf, d1); else d0 = mfma16(w[i >> 1], xf, d0);
    });
    {
      unsigned char* dst = vf + (wave >> 1) * 1024 + (n + 16 * (2 * (wave & 1) + (g >> 1))) * 16 + (g & 1) * 8;
      *(u32x2*)dst = (u32x2){pk2(d0[0], d0[1]), pk2(d0[2], d0[3])};
      *(u32x2*)(dst + 4096) = (u32x2){pk2(d1[0], d1[1]), pk2(d1[2], d1[3])};
    }
    FSTAMP(0);
    BAR_LDS();
    u32x4 va[8];
#pragma unroll
    for (int i = 0; i < 8; i++) va[i] = *(const u32x4*)(vf + i * 1024 + lane * 16);
    SCHED();
    const unsigned b = OFF_ZT + (unsigned)(s * NH + h) * (80u * 512u) + wave * 1024u;
    const unsigned lo = ((n >> 1) & 1) * 512u + g * 128u + (2 * (n >> 2) + (n & 1)) * 16u;
#pragma unroll
    for (int t = 0; t < 5; t++) {
      f32x4 z0 = {0, 0, 0, 0}, z1 = {0, 0, 0, 0};
#pragma unroll
      for (int ke = 0; ke < 4; ke++) {
        z0 = mfma16(va[ke], w[20 + t * 4 + ke], z0);
        z1 = mfma16(va[4 + ke], w[20 + t * 4 + ke], z1);
      }
      st16c((u32x4){pk2(z0[0], z0[1]), pk2(z0[2], z0[3]), pk2(z1[0], z1[1]), pk2(z1[2], z1[3])}, r, lo, b + t * 8192u);
    }
    FSTAMP(1);
    seg_end<5>(c, j, C_PVZ + par * 32 + h);
    FSTAMP(2);
    FSAVE(j);
    STAMP(5, c.role, 16 + j);
  }
  FDUMP();
}

DEV void role_qkv(const Ctx& c, int a) {
  const int lane = c.lane, wave = c.wave, n = lane & 15, g = lane >> 4;
  const rsrc_t r = c.r;
  const int cgp = a & 7, tg = a >> 3, s0 = tg * NSEG / 2, s1 = (tg + 1) * NSEG / 2;
  const int sl = cgp * 8 + wave;
  const bool act = sl < 60;
  u32x4 w[40];
  load_weights<1>(c, w, c.p.wqkv + cgp * 256, 1920, 128 * 1920, c.p.wqkv + cgp * 256 + (cgp < 7 ? 128 : 0), 1920, 128 * 1920, s0);
  BAR_ALL();
  for (int s = s0; s < s1; s++) {
    if (s + 1 < s1) stage_x(c, s + 1, (s + 1) & 1);
    const unsigned char* xb = c.lds + ((s & 1) ? XB1 : XB0) + lane * 16;
    if (act) {
#pragma unroll
      for (int rt = 0; rt < 2; rt++) {
        f32x4 d[2] = {{0, 0, 0, 0}, {0, 0, 0, 0}};
        if (sl < 40) {
          piped<20, 8>([&](int i) { return xb + (rt * 20 + i) * 1024; }, [&](int i, const u32x4& xf) {
            d[0] = mfma16(w[i], xf, d[0]);
            d[1] = mfma16(w[20 + i], xf, d[1]);
          });
#pragma unroll
          for (int t = 0; t < 2; t++) {
            const int col = sl * 32 + t * 16 + 4 * g, isk = col >= 640, cc = col - isk * 640, hh = cc / 20, dd = cc - hh * 20, tok = rt * 16 + n;
            const unsigned o = dd < 16 ? (unsigned)((tok + 32 * (dd >> 3)) * 16 + (dd & 7) * 2) : (unsigned)(1024 + tok * 8);
            const unsigned b = isk ? OFF_KV + (unsigned)(hh * 64 + s) * KVT : OFF_QA + (unsigned)(hh * 64 + s) * QAT;
            st8c((u32x2){pk2(d[t][0], d[t][1]), pk2(d[t][2], d[t][3])}, r, b + o, 0);
          }
        } else {
          piped<20, 8>([&](int i) { return xb + (rt * 20 + i) * 1024; }, [&](int i, const u32x4& xf) {
            d[0] = mfma16(xf, w[i], d[0]);
            d[1] = mfma16(xf, w[20 + i], d[1]);
          });
#pragma unroll
          for (int t = 0; t < 2; t++) {
            const int cc = (sl - 40) * 32 + t * 16 + n, hh = cc / 20, dd = cc - hh * 20;
            const unsigned o = 1280u + (unsigned)((rt * 2 + (g & 1)) * 384 + dd * 16 + (g >> 1) * 8);
            st8c((u32x2){pk2(d[t][0], d[t][1]), pk2(d[t][2], d[t][3])}, r, OFF_KV + (unsigned)(hh * 64 + s) * KVT + o, 0);
          }
        }
      }
    }
    BAR_ALL();
  }
  if (wave == 0 && lane == 0) atomic_inc(c.p.ws, C_QKVDONE);
}

DEV int take_ticket(const Ctx& c, int line) {
  unsigned* slot = (unsigned*)(c.lds + 160 * 1024 - 16);
  BAR_LDS();
  if (c.wave == 0 && c.lane == 0) *slot = atomic_inc(c.p.ws, line);
  BAR_ALL();
  return __builtin_amdgcn_readfirstlane(*slot);
}

DEV void attn_unit(const Ctx& c, int h, int qb) {
  const int lane = c.lane, wave = c.wave, tid = wave * 64 + lane, q = lane & 31, hf = lane >> 5;
  const rsrc_t r = c.r;
  constexpr int CH = 8 * KVT;
  const unsigned qo = OFF_QA + (unsigned)(h * 64 + qb * 8 + wave) * QAT;
  const u32x4 q0 = ld16c(r, lane * 16u, qo);
  u32x2 q1 = ld8c(r, 1024u + q * 8u, qo);
  if (hf) q1 = (u32x2){0, 0};
  const unsigned kvo = OFF_KV + (unsigned)(h * 64) * KVT;
  const bool third = tid < (CH / 16 - 1024);
  u32x4 st0 = ld16c(r, tid * 16u, kvo), st1 = ld16c(r, tid * 16u + 8192u, kvo), st2 = ld16c(r, (third ? tid : 0) * 16u + 16384u, kvo);
  *(u32x4*)(c.lds + tid * 16) = st0; *(u32x4*)(c.lds + tid * 16 + 8192) = st1;
  if (third) *(u32x4*)(c.lds + tid * 16 + 16384) = st2;
  BAR_LDS();
  const float cs = 0.22360679774997896f * 1.4426950408889634f;
  f32x16 o;
#pragma unroll
  for (int i = 0; i < 16; i++) o[i] = 0;
  float m = -1.0e30f;
  const int vo = 1280 + hf * 384 + (q < 20 ? q : 20) * 16;
  for (int ch = 0; ch < 8; ch++) {
    if (ch + 1 < 8) {
      const unsigned b = kvo + (unsigned)(ch + 1) * CH;
      st0 = ld16c(r, tid * 16u, b); st1 = ld16c(r, tid * 16u + 8192u, b); st2 = ld16c(r, (third ? tid : 0) * 16u + 16384u, b);
    }
    const unsigned char* kb = c.lds + (ch & 1) * CH;
#pragma unroll 1
    for (int kp = 0; kp < 4; kp++) {
      f32x16 sc[2];
#pragma unroll
      for (int t = 0; t < 2; t++) {
        const unsigned char* kt = kb + (kp * 2 + t) * KVT;
        const u32x4 k0 = *(const u32x4*)(kt + lane * 16);
        const u32x2 k1 = *(const u32x2*)(kt + 1024 + q * 8);
        f32x16 z;
#pragma unroll
        for (int i = 0; i < 16; i++) z[i] = 0;
        z = __builtin_amdgcn_mfma_f32_32x32x16_bf16(as8(k0), as8(q0), z, 0, 0, 0);
        sc[t] = __builtin_amdgcn_mfma_f32_32x32x8bf16_1k(__builtin_bit_cast(s16x4, k1), __builtin_bit_cast(s16x4, q1), z, 0, 0, 0);
      }
      float mx = sc[0][0];
#pragma unroll
      for (int i = 1; i < 16; i++) mx = fmaxf(mx, sc[0][i]);
#pragma unroll
      for (int i = 0; i < 16; i++) mx = fmaxf(mx, sc[1][i]);
      mx = fmaxf(mx, xor32(mx));
      if (__any((mx - m) * cs > 8.0f)) {
        const float mn = fmaxf(m, mx), al = __builtin_amdgcn_exp2f((m - mn) * cs);
#pragma unroll
        for (int i = 0; i < 12; i++) o[i] *= al;
        m = mn;
      }
      const float mc = -m * cs;
#pragma unroll
      for (int t = 0; t < 2; t++) {
        const unsigned char* kt = kb + (kp * 2 + t) * KVT;
#pragma unroll
        for (int s2 = 0; s2 < 2; s2++) {
          float e[8];
#pragma unroll
          for (int j = 0; j < 8; j++) e[j] = __builtin_amdgcn_exp2f(__builtin_fmaf(sc[t][8 * s2 + j], cs, mc));
          const u32x4 pf = {pk2(e[0], e[1]), pk2(e[2], e[3]), pk2(e[4], e[5]), pk2(e[6], e[7])};
          const u32x4 vfr = *(const u32x4*)(kt + vo + s2 * 768);
          o = __builtin_amdgcn_mfma_f32_32x32x16_bf16(as8(vfr), as8(pf), o, 0, 0, 0);
        }
      }
    }
    if (ch + 1 < 8) {
      unsigned char* nb = c.lds + ((ch + 1) & 1) * CH;
      *(u32x4*)(nb + tid * 16) = st0; *(u32x4*)(nb + tid * 16 + 8192) = st1;
      if (third) *(u32x4*)(nb + tid * 16 + 16384) = st2;
    }
    BAR_LDS();
  }
  const float lo = xor32(o[8]);
  const float inv = 1.0f / (hf ? o[8] : lo);
  const unsigned ob = OFF_OF + (unsigned)(qb * 8 + wave) * 40960u, ol = (q >> 4) * 20480u + (q & 15) * 16u;
#pragma unroll
  for (int b = 0; b < 3; b++) {
    const int col = h * 20 + 8 * b + 4 * hf;
    if (b < 2 || !hf)
      st8c((u32x2){pk2(o[4 * b] * inv, o[4 * b + 1] * inv), pk2(o[4 * b + 2] * inv, o[4 * b + 3] * inv)}, r,
           ol + (unsigned)((col >> 5) * 1024 + ((col >> 3) & 3) * 256 + (col & 7) * 2), ob);
  }
  BAR_ALL();
  if (wave == 0 && lane == 0) atomic_inc(c.p.ws, C_AT + qb);
}

DEV void final_unit(const Ctx& c, int seg, int cb) {
  const int lane = c.lane, wave = c.wave, tid = wave * 64 + lane, n = lane & 15, g = lane >> 4;
  const rsrc_t r = c.r;
  const int T = cb * 8 + wave;
  f32x4 a0 = {0, 0, 0, 0}, a1 = {0, 0, 0, 0};
  {
    u32x4 wf[20];
#pragma unroll
    for (int i = 0; i < 20; i++) wf[i] = ld16(r, lane * 16u + (i & 3) * 1024u, OFF_WO + (unsigned)T * 20480u + (i >> 2) * 4096u);
    if (wave == 0) { wait_lines(r, c.budget, C_AT + (seg >> 3), 1, 32, lane, 1); wait_lines(r, c.budget, C_PVZ + (seg & 1) * 32, 32, (seg >> 1) + 1, lane, 1); }
    BAR_LDS();
    u32x4 t[5];
#pragma unroll
    for (int i = 0; i < 5; i++) t[i] = ld16c(r, tid * 16u, OFF_OF + (unsigned)seg * 40960u + i * 8192u);
#pragma unroll
    for (int i = 0; i < 5; i++) *(u32x4*)(c.lds + i * 8192 + tid * 16) = t[i];
    BAR_LDS();
#pragma unroll
    for (int ks = 0; ks < 20; ks++) {
      a0 = mfma16(wf[ks], *(const u32x4*)(c.lds + ks * 1024 + lane * 16), a0);
      a1 = mfma16(wf[ks], *(const u32x4*)(c.lds + (20 + ks) * 1024 + lane * 16), a1);
    }
  }
  const unsigned cgo = (unsigned)(2 * T + ((n >> 1) & 1)), cc = (unsigned)(2 * (n >> 2) + (n & 1));
  u32x4 zt[32];
#pragma unroll
  for (int h = 0; h < 32; h++) zt[h] = ld16c(r, cgo * 512u + g * 128u + cc * 16u, OFF_ZT + (unsigned)(seg * NH + h) * (80u * 512u));
  const float g0 = *(const float*)(c.p.ws + OFF_GATE + (seg * 32 + n) * 4), g1 = *(const float*)(c.p.ws + OFF_GATE + (seg * 32 + 16 + n) * 4);
  if (wave == 0) { wait_lines(r, c.budget, C_PCH, 1, seg, lane, 1); wait_lines(r, c.budget, C_PCH, 64, seg + 1, lane, 0); wait_lines(r, c.budget, C_PCH + 64, 48, seg + 1, lane, 0); }
  BAR_LDS();
  unsigned char* pl = c.lds + 40960;
  u32x4 zm[8];
  {
    u32x4 t[10];
#pragma unroll
    for (int i = 0; i < 10; i++) t[i] = ld16c(r, tid * 16u, OFF_PT + (unsigned)seg * 81920u + i * 8192u);
#pragma unroll
    for (int i = 0; i < 8; i++) zm[i] = ld16c(r, (g * 80u + cgo) * 128u + cc * 16u, OFF_ZM + (unsigned)(seg * NH + 4 * i) * (80u * 128u));
#pragma unroll
    for (int i = 0; i < 10; i++) *(u32x4*)(pl + i * 8192 + tid * 16) = t[i];
  }
  BAR_LDS();
  f32x4 b0 = {0, 0, 0, 0}, b1 = {0, 0, 0, 0};
#pragma unroll
  for (int h = 0; h < 32; h++) {
    b0 = mfma16(zt[h], *(const u32x4*)(pl + (h * 2) * PTT + lane * 16), b0);
    b1 = mfma16(zt[h], *(const u32x4*)(pl + (h * 2 + 1) * PTT + lane * 16), b1);
  }
#pragma unroll
  for (int i = 0; i < 8; i++) {
    b0 = mfma16(zm[i], *(const u32x4*)(pl + ((4 * i + g) * 2) * PTT + 1024 + n * 16), b0);
    b1 = mfma16(zm[i], *(const u32x4*)(pl + ((4 * i + g) * 2 + 1) * PTT + 1024 + n * 16), b1);
  }
  const float h0 = rbf(1.f - g0), h1 = rbf(1.f - g1);
  float y0[4], y1[4];
#pragma unroll
  for (int j = 0; j < 4; j++) {
    y0[j] = rbf(g0 * rbf(a0[j])) + rbf(h0 * rbf(b0[j]));
    y1[j] = rbf(g1 * rbf(a1[j])) + rbf(h1 * rbf(b1[j]));
  }
  bf16* o = c.p.out + (size_t)(seg * 32 + n) * DM + T * 16 + 4 * g;
  *(u32x2*)o = (u32x2){pk2(y0[0], y0[1]), pk2(y0[2], y0[3])};
  *(u32x2*)(o + 16 * DM) = (u32x2){pk2(y1[0], y1[1]), pk2(y1[2], y1[3])};
}

__global__ void __launch_bounds__(512, 2) main_kernel(Params p) {
  extern __shared__ __attribute__((aligned(16))) unsigned char lds[];
  const int lane = threadIdx.x & 63, wave = __builtin_amdgcn_readfirstlane(threadIdx.x >> 6);
  Ctx c{p, make_rsrc(p.ws, WS_USED), lds, lane, wave, 0, SPIN_CAP};
  unsigned* sl = (unsigned*)(lds + 160 * 1024 - 16);
  if (wave == 0) {
    const unsigned x = xcc_id();
    unsigned t = 0;
    if (lane == 0) t = atomic_inc(p.ws, C_TICKET + x);
    t = __builtin_amdgcn_readfirstlane(t);
    unsigned role = x * 32 + t;
    if (t >= 32) {
#pragma unroll 1
      for (;;) {
        FENCE();
        unsigned v = ld4c(c.r, OFF_CTRL + (C_TICKET + (lane & 7)) * 128, 0);
        v += __shfl_xor(v, 1); v += __shfl_xor(v, 2); v += __shfl_xor(v, 4);
        if (__builtin_amdgcn_readfirstlane(v) >= gridDim.x || --c.budget < 0) break;
      }
      unsigned k = 0;
      if (lane == 0) k = atomic_inc(p.ws, C_OVF);
      k = __builtin_amdgcn_readfirstlane(k);
      role = 0xFFFFu;
      for (int y = 0; y < 8; y++) {
        const unsigned cnt = __builtin_amdgcn_readfirstlane(ld4c(c.r, OFF_CTRL + (C_TICKET + y) * 128, 0));
        const unsigned vac = cnt < 32 ? 32 - cnt : 0;
        if (role == 0xFFFFu) { if (k < vac) role = y * 32 + cnt + k; else k -= vac; }
      }
    }
    if (lane == 0) *sl = role;
  }
  BAR_ALL();
  const int role = __builtin_amdgcn_readfirstlane(*sl);
  const int grp = role >> 5, slot = role & 31;
  if (grp > 7) return;
  c.role = role;
  STAMP(5, c.role, 1);
  const unsigned comm = comm_start(p.ws);
  const unsigned pol = (unsigned)__builtin_amdgcn_ballot_w64(lane < NPAGE && *(const unsigned*)(p.ws + OFF_CTRL + (C_POL + (lane & 7)) * 128) == 1u);
  BAR_LDS();
  if (grp < 4) make_vo(c, role);
  STAMP(5, c.role, 8);
  if (grp == 0) { role_S(c, slot, comm, pol); return; }
  if (grp < 4) {
    const int xc = (grp - 1) * 27 + slot;
    if (slot < 27 && xc < 80) { role_X(c, xc, comm, pol); return; }
    role_qkv(c, (grp - 1) * 5 + (grp == 3 ? slot - 26 : slot - 27));
  } else if (slot & 8) role_VZ(c, (grp - 4) * 8 + (slot & 7), slot >> 4);
  else role_QK(c, (grp - 4) * 8 + (slot & 7), slot >> 4);
  STAMP(5, c.role, 2);
  if (wave == 0) wait_lines(c.r, c.budget, C_QKVDONE, 1, 16, lane, 1);
  STAMP(0, c.role, 3);
  for (;;) {
    const int u = take_ticket(c, C_QATT);
    if (u >= 256) break;
    attn_unit(c, u & 31, u >> 5);
    STAMP(5, 256, u);
  }
  STAMP(5, c.role, 4);
  for (;;) {
    const int u = take_ticket(c, C_QFIN);
    if (u >= NSEG * 5) break;
    final_unit(c, u / 5, u % 5);
    STAMP(5, 257, u);
  }
  STAMP(5, c.role, 5);
}

extern "C" void kernel_launch(void* const* d_in, const int* in_sizes, int n_in, void* d_out, int out_size, void* d_ws, size_t ws_size,
                              hipStream_t stream) {
  Params p;
  p.x = (const bf16*)d_in[0]; p.wqkv = (const bf16*)d_in[1]; p.wo = (const bf16*)d_in[2]; p.mem0 = (const bf16*)d_in[4];
  p.Wq = (const bf16*)d_in[5]; p.Wk = (const bf16*)d_in[6]; p.Wv = (const bf16*)d_in[7]; p.Wo = (const bf16*)d_in[8];
  p.gw = (const bf16*)d_in[10]; p.out = (bf16*)d_out; p.ws = (unsigned char*)d_ws;
  hipFuncSetAttribute((const void*)main_kernel, hipFuncAttributeMaxDynamicSharedMemorySize, 160 * 1024);
  prep_kernel<<<256, 512, 0, stream>>>(p);
  main_kernel<<<256, 512, 160 * 1024, stream>>>(p);
}
```

```cpp
#include <hip/hip_runtime.h>
#include <hip/hip_bf16.h>

typedef __bf16 bf16;
typedef __attribute__((ext_vector_type(2))) __bf16 bf16x2;
typedef __attribute__((ext_vector_type(8))) __bf16 bf16x8;
typedef __attribute__((ext_vector_type(16))) __bf16 bf16x16;
typedef __attribute__((ext_vector_type(4))) short s16x4;
typedef __attribute__((ext_vector_type(2))) float f32x2;
typedef __attribute__((ext_vector_type(4))) float f32x4;
typedef __attribute__((ext_vector_type(16))) float f32x16;
typedef __attribute__((ext_vector_type(2))) unsigned u32x2;
typedef __attribute__((ext_vector_type(4))) unsigned u32x4;
typedef __attribute__((ext_vector_type(8))) unsigned u32x8;
typedef __amdgpu_buffer_rsrc_t rsrc_t;
#define DEV __device__ __forceinline__
#define SC1 16

constexpr int NSEG = 64, NH = 32, DM = 640;
constexpr unsigned KB = 1024u, MB = 1024u * 1024u;
constexpr unsigned OFF_CTRL = 0;
constexpr unsigned OFF_GATE = 64 * KB;
constexpr unsigned OFF_WO = 1 * MB;
constexpr unsigned OFF_OF = 2 * MB;
constexpr unsigned OFF_QA = 5 * MB;
constexpr unsigned OFF_KV = 8 * MB;
constexpr unsigned OFF_PT = 14 * MB;
constexpr unsigned OFF_COMM = 20 * MB;
constexpr unsigned OFF_QKT = 26 * MB;
constexpr unsigned OFF_ZM = 58 * MB;
constexpr unsigned OFF_ZT = 78 * MB;
constexpr unsigned WS_USED = 158 * MB;
constexpr int C_TICKET = 0;
constexpr int C_OVF = 8, C_QKVDONE = 9, C_QATT = 10, C_QFIN = 11;
constexpr int C_FILL = 12, C_POL = 13;
constexpr int C_AT = 16;
constexpr int C_PQK = 32;
constexpr int C_PVZ = 64;
constexpr int C_PCH = 96;
constexpr int C_LINES = 160;
constexpr int NA = 88, NX = 40;
constexpr unsigned SENT = 0xFFFFFFFFu;
constexpr int SPIN_CAP = 1 << 21;
constexpr int KVT = 2816, QAT = 1280, PTT = 1280;
constexpr unsigned CM_M = 0, CM_P = 10240, CM_Z = 65536, CM_REP = 4 * 16384, CM_REP3 = 48 * 16384  ;
DEV unsigned comm_off(unsigned o) { return ((o >> 13) << 14) | (o & 8191u); }
DEV unsigned comm_base(unsigned comm, unsigned pol, int s) {
  const unsigned pg = ((unsigned)s >> 1) & 1u;
  return comm + pg * 2u * MB + ((unsigned)s & 1u) * 50u * 16384u + (((pol >> pg) & 1u) << 13);
}
DEV unsigned pace_addr(unsigned comm, unsigned pol, int s) { return comm + 100u * 16384u + ((pol & 1u) << 13) + (unsigned)s * 128u; }
#define TUNEV(i, d) (d)

struct Params {
  const bf16 *x, *wqkv, *wo, *mem0, *Wq, *Wk, *Wv, *Wo, *gw;
  bf16* out;
  unsigned char* ws;
};
struct Ctx {
  const Params& p;
  rsrc_t r;
  unsigned char* lds;
  int lane, wave, role;
  mutable int budget;
};

DEV rsrc_t make_rsrc(const void* p, unsigned bytes) { return __builtin_amdgcn_make_buffer_rsrc((void*)p, 0, bytes, 0x00020000); }
DEV unsigned comm_start(const unsigned char* ws) {
  const unsigned lo = (unsigned)((unsigned long long)ws & (2u * MB - 1));
  return OFF_COMM + ((2u * MB - lo) & (2u * MB - 1));
}
DEV unsigned xcc_id() { return (unsigned)__builtin_amdgcn_s_getreg(20 | (3 << 11)) & 7u; }
DEV unsigned pk2(float a, float b) { bf16x2 v = {(bf16)a, (bf16)b}; return __builtin_bit_cast(unsigned, v); }
DEV unsigned nosent(unsigned v) { return v < 0xFFFFFFFEu ? v : 0xFFFFFFFEu; }
DEV float rbf(float a) { return (float)(bf16)a; }
DEV bf16x8 as8(u32x4 v) { return __builtin_bit_cast(bf16x8, v); }
DEV bf16x16 cat16(u32x4 a, u32x4 b) { u32x8 v = {a.x, a.y, a.z, a.w, b.x, b.y, b.z, b.w}; return __builtin_bit_cast(bf16x16, v); }
DEV f32x4 mfma16(u32x4 a, u32x4 b, f32x4 c) { return __builtin_amdgcn_mfma_f32_16x16x32_bf16(as8(a), as8(b), c, 0, 0, 0); }
DEV f32x4 mfma8(u32x4 a, u32x4 b0, u32x4 b1, f32x4 c, int idx) { return __builtin_amdgcn_smfmac_f32_16x16x64_bf16(as8(a), cat16(b0, b1), c, idx, 0, 0); }
DEV u32x4 ld16c(rsrc_t r, unsigned v, unsigned s) { return __builtin_amdgcn_raw_buffer_load_b128(r, v, s, SC1); }
DEV u32x2 ld8c(rsrc_t r, unsigned v, unsigned s) { return __builtin_amdgcn_raw_buffer_load_b64(r, v, s, SC1); }
DEV unsigned ld4c(rsrc_t r, unsigned v, unsigned s) { return __builtin_amdgcn_raw_buffer_load_b32(r, v, s, SC1); }
DEV void st16c(u32x4 d, rsrc_t r, unsigned v, unsigned s) {
  __builtin_amdgcn_raw_buffer_store_b128(d, r, v, s, SC1);
  asm volatile("s_nop 1" ::"v"(d) : "memory");
}
DEV void st8c(u32x2 d, rsrc_t r, unsigned v, unsigned s) { __builtin_amdgcn_raw_buffer_store_b64(d, r, v, s, SC1); }
DEV void st4c(unsigned d, rsrc_t r, unsigned v, unsigned s) { __builtin_amdgcn_raw_buffer_store_b32(d, r, v, s, SC1); }
DEV unsigned umax(unsigned a, unsigned b) { return a > b ? a : b; }
DEV unsigned max4(u32x4 v) { return umax(umax(umax(v.x, v.y), v.z), v.w); }
DEV bool any_unwritten(unsigned m, int& budget) {
  const unsigned thr = --budget >= 0 ? SENT - 1u : SENT;
  return __builtin_amdgcn_ballot_w64(m > thr) != 0;
}
#define FENCE() asm volatile("" ::: "memory")
#define BAR_LDS() asm volatile("s_waitcnt lgkmcnt(0)\n\ts_barrier" ::: "memory")
#define BAR_ALL() asm volatile("s_waitcnt vmcnt(0) lgkmcnt(0)\n\ts_barrier" ::: "memory")
#define WAIT_BAR(n) asm volatile("s_waitcnt vmcnt(%0) lgkmcnt(0)\n\ts_barrier" ::"n"(n) : "memory")
#define DRAIN() asm volatile("s_waitcnt vmcnt(0)" ::: "memory")
#define SCHED() __builtin_amdgcn_sched_barrier(0)
constexpr u32x4 MARK = {SENT, SENT, SENT, SENT};
DEV float vmax(float a, float b) { float d; asm("v_max_f32 %0, %1, %2" : "=v"(d) : "v"(a), "v"(b)); return d; }
DEV float max_halves(float v) {
  auto b = __builtin_amdgcn_permlane32_swap(__float_as_uint(v), __float_as_uint(v), false, false);
  return vmax(__uint_as_float(b[0]), __uint_as_float(b[1]));
}
DEV float max_rows(float v) {
  auto a = __builtin_amdgcn_permlane16_swap(__float_as_uint(v), __float_as_uint(v), false, false);
  return max_halves(vmax(__uint_as_float(a[0]), __uint_as_float(a[1])));
}
DEV float sum_rows(float v) {
  auto a = __builtin_amdgcn_permlane16_swap(__float_as_uint(v), __float_as_uint(v), false, false);
  v = __uint_as_float(a[0]) + __uint_as_float(a[1]);
  auto b = __builtin_amdgcn_permlane32_swap(__float_as_uint(v), __float_as_uint(v), false, false);
  return __uint_as_float(b[0]) + __uint_as_float(b[1]);
}
DEV float dpp_ror8(float v) { return __uint_as_float(__builtin_amdgcn_update_dpp(0u, __float_as_uint(v), 0x128, 0xf, 0xf, true)); }
DEV unsigned atomic_inc(unsigned char* ws, int line) {
  return __hip_atomic_fetch_add((unsigned*)(ws + OFF_CTRL + line * 128), 1u, __ATOMIC_RELAXED, __HIP_MEMORY_SCOPE_AGENT);
}
DEV unsigned wait_lines(const Ctx& c, int first, int n, unsigned want, int nap) {
  const unsigned a = OFF_CTRL + (unsigned)(first + (c.lane < n ? c.lane : 0)) * 128u;
  unsigned v;
#pragma unroll 1
  for (;;) {
    FENCE();
    v = ld4c(c.r, a, 0);
    if (__builtin_amdgcn_ballot_w64(v < want) == 0 || --c.budget < 0) break;
    if (nap) __builtin_amdgcn_s_sleep(8);
  }
  FENCE();
  return v;
}
struct Prog {
  unsigned v, addr;
  DEV void init(int first, int n, int lane) { v = 0; addr = OFF_CTRL + (unsigned)(first + (lane & (n - 1))) * 128u; }
  DEV bool lacks(int s) const { return __builtin_amdgcn_ballot_w64(v <= (unsigned)s) != 0; }
  DEV void need(const Ctx& c, int s) {
    if (lacks(s)) {
      bool again;
#pragma unroll 1
      do {
        FENCE();
        v = ld4c(c.r, addr, 0);
        asm volatile("" : "+v"(v));
        const bool more = --c.budget >= 0;
        again = lacks(s) & more;
      } while (again);
    }
    FENCE();
  }
  DEV void refresh(const Ctx& c, int s) { if (s < NSEG && lacks(s)) v = ld4c(c.r, addr, 0); }
};
DEV unsigned now() { unsigned long long t; asm volatile("s_memtime %0\n\ts_waitcnt lgkmcnt(0)" : "=s"(t)::"memory"); return (unsigned)t; }
struct Clock {
  unsigned r0, c0;
  DEV static void read(unsigned& r, unsigned& cy) {
    unsigned long long a, b;
    asm volatile("s_memrealtime %0\n\ts_memtime %1\n\ts_waitcnt lgkmcnt(0)" : "=&s"(a), "=&s"(b)::"memory");
    r = (unsigned)a * 10u; cy = (unsigned)b;
  }
  DEV void init() { read(r0, c0); r0 += 20u; }
  DEV void align() {
    unsigned r, pr, cy;
    read(pr, cy);
    r = pr;
#pragma unroll
    for (int i = 0; i < 4; i++) if (r == pr) read(r, cy);
    r0 = r + 8u; c0 = cy;
  }
  DEV static unsigned ns(unsigned cycles) { return (cycles * 445u) >> 10; }
  DEV unsigned at(unsigned cy) const { return r0 + ns(cy - c0); }
  DEV unsigned cyc(unsigned t) const {
    const int rem = (int)(t - r0);
    return c0 + (unsigned)(((rem > 4000 ? 4000 : rem < 0 ? 0 : rem) * 2355) >> 10);
  }
  DEV static void spin_cyc(unsigned ct) {
#pragma unroll 1
    for (int i = 0; i < 40; i++)
      if ((int)(now() - ct) >= 0 || (int)(now() - ct) >= 0 || (int)(now() - ct) >= 0 || (int)(now() - ct) >= 0 || (int)(now() - ct) >= 0 || (int)(now() - ct) >= 0) break;
  }
  DEV void spin(unsigned t) const { spin_cyc(cyc(t)); }
  DEV void wait(unsigned t) {
    unsigned r, cy;
    read(r, cy);
    if ((int)(r + 160u - t) < 0) {
#pragma unroll 1
      for (int i = 0; i < 256; i++) {
        const unsigned pr = r;
        read(r, cy);
        if (r == pr) read(r, cy);
        if (r == pr) read(r, cy);
        if (r != pr) { r0 = r + 8u; c0 = cy; }
        if ((int)(r + 200u - t) >= 0) break;
      }
    } else if (cy - c0 > 6000u) { r0 = r + 20u; c0 = cy; }
    spin(t);
  }
};
#define STAMP(w, role, idx) do {} while (0)
#define FDECL() do {} while (0)
#define FSTAMP(k) do {} while (0)
#define FSAVE(s) do {} while (0)
#define FDUMP() do {} while (0)
#define GSTAMP(s, k) do {} while (0)
DEV void glds16(const void* g, unsigned lds_dst) {
  unsigned keep;
  asm volatile("s_mov_b32 %0, m0\n\ts_mov_b32 m0, %2\n\ts_nop 0\n\tglobal_load_lds_dwordx4 %1, off\n\ts_mov_b32 m0, %0"
               : "=&s"(keep) : "v"(g), "s"(lds_dst) : "memory");
}
DEV void glds16w(const void* g, unsigned lds_dst) {
  unsigned keep;
  asm volatile("s_mov_b32 %0, m0\n\ts_mov_b32 m0, %2\n\ts_nop 0\n\tglobal_load_lds_dwordx4 %1, off\n\ts_mov_b32 m0, %0"
               : "=&s"(keep) : "v"(g), "s"(lds_dst) : "memory");
}
DEV unsigned lds_addr(const void* p) { return (unsigned)(unsigned long long)(const __attribute__((address_space(3))) void*)p; }

constexpr unsigned XB0 = 65536, XB1 = 0;
DEV void stage_x(const Ctx& c, int seg, int buf) {
  const bf16* src = c.p.x + (size_t)(seg * 32 + (c.wave >> 1) * 8 + (c.lane >> 3)) * DM + (c.wave & 1) * 320 + ((c.lane & 7) ^ (c.lane >> 3)) * 8;
  const unsigned dst = lds_addr(c.lds) + (buf ? XB1 : XB0) + c.wave * 5120;
#pragma unroll
  for (int i = 0; i < 5; i++) glds16(src + i * 64, dst + i * 1024);
}
struct XTile {
  const unsigned char *e, *o;
  DEV XTile(const Ctx& c, int buf) {
    const int n = c.lane & 15, g = c.lane >> 4;
    const unsigned b = (buf ? XB1 : XB0) + (n >> 3) * 10240 + (n & 7) * 128;
    e = c.lds + b + ((g ^ (n & 7)) << 4);
    o = c.lds + b + (((4 + g) ^ (n & 7)) << 4);
  }
  DEV const unsigned char* at(int rt, int ks) const { return ((ks & 1) ? o : e) + rt * 20480 + (ks >> 1) * 1024; }
};
DEV void dma_block(const Ctx& c, const bf16* src, int ld, unsigned lds_off) {
  const int q = c.lane >> 4;
#pragma unroll
  for (int i = 0; i < 4; i++)
    glds16w(src + (size_t)(c.wave * 16 + i * 4 + q) * ld + (((c.lane & 15) ^ ((q << 2) | i)) << 3), lds_addr(c.lds) + lds_off + (c.wave * 16 + i * 4) * 256);
}
typedef __attribute__((address_space(3))) s16x4* lds_s16x4;
DEV void read_cols(const Ctx& c, unsigned lds_off, int col0, int cstep, u32x4* f) {
  const int g = c.lane >> 4, q = (c.lane >> 2) & 3, col = col0 + cstep * (c.lane & 3);
  const unsigned a0 = lds_addr(c.lds) + lds_off + (8 * g + q) * 256 + (((col >> 3) ^ ((q << 2) | ((2 * g) & 3))) << 4) + ((col >> 2) & 1) * 8;
  const unsigned a1 = lds_addr(c.lds) + lds_off + (8 * g + 4 + q) * 256 + (((col >> 3) ^ ((q << 2) | ((2 * g + 1) & 3))) << 4) + ((col >> 2) & 1) * 8;
#pragma unroll
  for (int ks = 0; ks < 4; ks++) {
    const u32x2 lo = __builtin_bit_cast(u32x2, __builtin_amdgcn_ds_read_tr16_b64_v4i16((lds_s16x4)(size_t)(a0 + ks * 8192)));
    const u32x2 hi = __builtin_bit_cast(u32x2, __builtin_amdgcn_ds_read_tr16_b64_v4i16((lds_s16x4)(size_t)(a1 + ks * 8192)));
    f[ks] = (u32x4){lo.x, lo.y, hi.x, hi.y};
  }
}
template <int MODE>
DEV void load_weights(const Ctx& c, u32x4 (&w)[40], const bf16* A, int lda, int stepa, const bf16* B, int ldb, int stepb, int xseg, bool plain = false) {
#pragma unroll
  for (int j = 0; j < 2; j++) { dma_block(c, A + (size_t)j * stepa, lda, j * 65536); dma_block(c, B + (size_t)j * stepb, ldb, j * 65536 + 32768); }
#pragma unroll
  for (int j = 0; j < 5; j++) {
    if (j < 4) WAIT_BAR(8); else if (xseg >= 0) WAIT_BAR(5); else WAIT_BAR(0);
    const unsigned o = (j & 1) * 65536;
    if (MODE == 2) {
      read_cols(c, o, 16 * c.wave, 4, &w[4 * j]);
      read_cols(c, o + 32768, 16 * c.wave, 4, &w[20 + 4 * j]);
    } else {
      read_cols(c, o + (c.wave >> 2) * 32768, 32 * (c.wave & 3), plain ? 4 : 8, &w[4 * j]);
      read_cols(c, o + (c.wave >> 2) * 32768, 32 * (c.wave & 3) + (plain ? 16 : 4), plain ? 4 : 8, &w[20 + 4 * j]);
    }
    BAR_LDS();
    if (j + 2 < 5) { dma_block(c, A + (size_t)(j + 2) * stepa, lda, o); dma_block(c, B + (size_t)(j + 2) * stepb, ldb, o + 32768); }
    if (j == 3 && xseg >= 0) stage_x(c, xseg, 0);
  }
#pragma unroll
  for (int i = 0; i < 40; i++) asm volatile("" : "+v"(w[i]));
}

DEV unsigned ring_ready(const Ctx& c) {
  wait_lines(c, C_FILL, 1, NX - 2, 0);
  const unsigned v = wait_lines(c, C_POL, 2, 1, 0);
  return (unsigned)__builtin_amdgcn_ballot_w64(c.lane < 2 && v == 2u);
}
struct Looks { DEV void retire() const {} };
DEV bool unwritten2(u32x4 a, u32x4 b) { return __builtin_amdgcn_ballot_w64(umax(max4(a), max4(b)) == SENT) != 0; }
DEV Looks gather2(const Ctx& c, unsigned base, unsigned a0, unsigned char* dst, int gap) {
  u32x4 v0 = ld16c(c.r, a0, base), v1 = ld16c(c.r, a0 + 1024u, base);
  if (__builtin_expect(unwritten2(v0, v1), 0)) {
    unsigned b = base + CM_REP;
#pragma unroll 1
    do {
      FENCE();
      v0 = ld16c(c.r, a0, b); v1 = ld16c(c.r, a0 + 1024u, b);
      b = base + CM_REP3;
    } while (any_unwritten(umax(max4(v0), max4(v1)), c.budget));
  }
  *(u32x4*)dst = v0;
  *(u32x4*)(dst + 1024) = v1;
  return Looks{};
}
DEV int key_token(int u, int m) { return 16 * ((m >> 2) & 1) + 8 * (m >> 3) + 4 * ((m >> 1) & 1) + 2 * u + (m & 1); }

template <int CLS>
DEV void role_S_loop(const Ctx& c, const u32x4 (&w)[40], int h, unsigned comm, unsigned pol) {
  const int lane = c.lane, wave = c.wave, n = lane & 15, g = lane >> 4;
  const rsrc_t r = c.r;
  const float cs = 0.08838834764831845f * 1.4426950408889634f;
  Prog pr; pr.init(C_PQK + h, 1, lane);
  const int delay = TUNEV(1, 2000), gap = TUNEV(5, 2);
  const int idx = (lane & 1) ? 0xEEEE : 0x4444;
  const unsigned ga0 = comm_off(CM_M + wave * 2048u + lane * 16u);
  const unsigned so0 = CLS == 0 ? comm_off(CM_P + h * 640u + ((g >> 1) * 16 + 2 * (n & 7) + (g & 1)) * 16u + (n >> 3) * 8u) : CLS == 4 ? comm_off(CM_P + h * 640u + lane * 16u) : lane * 16u;
  const unsigned so1 = CLS == 0 ? comm_off(CM_P + h * 640u + 512u + (n & 7) * 16u + (g & 1) * 8u + (g >> 1) * 4u) : 1024u + n * 16u + (g - 2) * 8u;
  constexpr int NT = CLS == 3 ? 5 : 6;
  const unsigned to = (wave - 5) * 5120u + lane * 16u;
  unsigned char* const gd = c.lds + wave * 2048 + lane * 16;
  const unsigned char* const asrc = c.lds + (2 * g + (lane & 1)) * 128 + (n >> 1) * 16;
  unsigned char* const xd = c.lds + 10240 + (wave & 3) * 1024 + ((wave >> 2) * 8 + 2 * g + 16 * (n >> 2)) * 16 + (n & 3) * 4;
  const unsigned char* const xq = c.lds + 10240 + ((n & 7) + 16 * g) * 16;
  u32x4 t[NT];
  if (CLS >= 3) {
    pr.need(c, 0);
#pragma unroll
    for (int i = 0; i < NT; i++) t[i] = ld16c(r, to + i * 1024u, OFF_QKT + (unsigned)h * 16u * KB);
#pragma unroll
    for (int i = 0; i < NT; i++) *(u32x4*)(c.lds + 18432 + to + i * 1024) = t[i];
  }
  if (CLS < 2) __builtin_amdgcn_s_setprio(2);
  Clock ck; unsigned cgo = 0, dur = 4000;
  if (CLS == 0) { ck.init(); cgo = ck.c0; }
  for (int s = 0; s < NSEG; s++) {
    FDECL();
    unsigned base = comm_base(comm, pol, s);
    unsigned o_tl = 18432 + (s & 1) * 16384 + (CLS < 2 ? 8192 + wave * 1024 + lane * 16 : 0);
    asm volatile("" : "+s"(base));
    if (CLS < 2) asm volatile("" : "+v"(o_tl));
    const unsigned char* tl = c.lds + o_tl;
    if (CLS == 4) GSTAMP(s, 2);
    if (CLS == 0) Clock::spin_cyc(cgo);
    FSTAMP(2);
    BAR_LDS();
    Looks lk;
    if (CLS < 3) lk = gather2(c, base, ga0, gd, gap);
    const bool early = CLS >= 3 && s + 1 < NSEG && !pr.lacks(s + 1);
    FSTAMP(0);
    BAR_LDS();
    f32x4 d0 = {0, 0, 0, 0}, d1 = {0, 0, 0, 0};
    {
      u32x4 a[10];
#pragma unroll
      for (int ks = 0; ks < 10; ks++) a[ks] = *(const u32x4*)(asrc + ks * 1024);
      SCHED();
      if (early) {
#pragma unroll
        for (int i = 0; i < NT; i++) t[i] = ld16c(r, to + i * 1024u, OFF_QKT + (unsigned)((s + 1) * NH + h) * 16u * KB);
        pr.refresh(c, s + 2);
      }
      SCHED();
#pragma unroll
      for (int ks = 0; ks < 10; ks++) {
        d0 = mfma8(a[ks], w[2 * ks], w[2 * ks + 1], d0, idx);
        d1 = mfma8(a[ks], w[20 + 2 * ks], w[21 + 2 * ks], d1, idx);
      }
      SCHED();
    }
    if (CLS < 3) lk.retire();
    u32x4 k0, k1;
    if (CLS < 2) { k0 = *(const u32x4*)tl; k1 = *(const u32x4*)(tl + 4096); }
    *(unsigned*)xd = pk2(d0[0] + d0[1], d1[0] + d1[1]);
    *(unsigned*)(xd + 16) = pk2(d0[2] + d0[3], d1[2] + d1[3]);
    if (CLS < 2) {
      FENCE();
      const u32x4 qf = *(const u32x4*)(xq + wave * 1024);
      const f32x4 s0 = mfma16(k0, qf, (f32x4){0, 0, 0, 0}), s1 = mfma16(k1, qf, (f32x4){0, 0, 0, 0});
      *(f32x4*)(c.lds + 14336 + wave * 1024 + lane * 16) = n < 8 ? s0 : s1;
    }
    FSTAMP(1);
    BAR_LDS();
    u32x4 pt; u32x2 pm;
    if (CLS == 0) {
      u32x4 kf[4], qf[4]; f32x4 pp[4];
#pragma unroll
      for (int ke = 0; ke < 4; ke++) { kf[ke] = *(const u32x4*)(xq + 128 + ke * 1024); qf[ke] = *(const u32x4*)(xq + ke * 1024); }
#pragma unroll
      for (int ke = 0; ke < 4; ke++) pp[ke] = *(const f32x4*)(c.lds + 14336 + ke * 1024 + lane * 16);
      SCHED();
      f32x4 sm = mfma16(kf[0], qf[0], (f32x4){0, 0, 0, 0});
#pragma unroll
      for (int ke = 1; ke < 4; ke++) sm = mfma16(kf[ke], qf[ke], sm);
      SCHED();
      const f32x4 st = (pp[0] + pp[1]) + (pp[2] + pp[3]);
      const float ma = n < 8 ? sm[0] : sm[1], mb = n < 8 ? sm[2] : sm[3], mv = g < 2 ? ma : mb;
      float mx = __builtin_fmaxf(__builtin_fmaxf(__builtin_fmaxf(st[0], st[1]), __builtin_fmaxf(st[2], st[3])), mv);
      mx = vmax(mx, dpp_ror8(mx));
      const float mc = -max_rows(mx) * cs;
      const float e0 = __builtin_amdgcn_exp2f(__builtin_fmaf(st[0], cs, mc)), e1 = __builtin_amdgcn_exp2f(__builtin_fmaf(st[1], cs, mc));
      const float e2 = __builtin_amdgcn_exp2f(__builtin_fmaf(st[2], cs, mc)), e3 = __builtin_amdgcn_exp2f(__builtin_fmaf(st[3], cs, mc));
      const float e4 = __builtin_amdgcn_exp2f(__builtin_fmaf(mv, cs, mc));
      const float e5 = dpp_ror8(e4);
      float sum = ((e0 + e1) + (e2 + e3)) + e4;
      sum += dpp_ror8(sum);
      const float inv = __builtin_amdgcn_rcpf(sum_rows(sum));
      u32x2 o0 = {nosent(pk2(e0 * inv, e1 * inv)), nosent(pk2(e2 * inv, e3 * inv))};
      unsigned o1 = nosent(pk2(e4 * inv, e5 * inv));
      asm volatile("" : "+v"(o0), "+v"(o1));
      st8c(o0, r, so0, base);
      if (n < 8) st4c(o1, r, so1, base);
      FSTAMP(3);
      st8c(o0, r, so0, base + CM_REP);
      if (n < 8) st4c(o1, r, so1, base + CM_REP);
      const unsigned cp = now();
      dur = min(dur + 8u, cp - cgo);
      cgo = cp + delay;
      if (h == 0) {
        ck.align();
        if (lane == 0) st4c(nosent(ck.at(cgo + dur)), r, pace_addr(comm, pol, s + 1), 0);
      }
    } else if (CLS == 3) {
      int rel = (wave - 5) * 16 + n - key_token(0, 4 * g);
      asm volatile("" : "+v"(rel));
      const float mneg = g >= 2 ? 0.f : -3.0e38f;
      f32x4 sm = {mneg, mneg, mneg, mneg}, s0, s1;
#pragma unroll
      for (int j = 0; j < 4; j++) { s0[j] = key_token(0, j) <= rel ? 0.f : -3.0e38f; s1[j] = key_token(1, j) <= rel ? 0.f : -3.0e38f; }
      const int kt = key_token(0, n);
      const unsigned char* kp = tl + 8192 + (kt >> 4) * 4096 + ((kt & 15) + 16 * g) * 16;
#pragma unroll
      for (int ke = 0; ke < 4; ke++) {
        const u32x4 qt = *(const u32x4*)(tl + (wave - 5) * 4096 + ke * 1024 + lane * 16);
        s0 = mfma16(*(const u32x4*)(kp + ke * 1024), qt, s0);
        s1 = mfma16(*(const u32x4*)(kp + ke * 1024 + 32), qt, s1);
        sm = mfma16(*(const u32x4*)(c.lds + 10240 + ke * 1024 + lane * 16), qt, sm);
      }
      float v[12];
#pragma unroll
      for (int j = 0; j < 4; j++) { v[j] = s0[j]; v[4 + j] = s1[j]; v[8 + j] = sm[j]; }
      float mx = v[0];
#pragma unroll
      for (int j = 1; j < 12; j++) mx = __builtin_fmaxf(mx, v[j]);
      const float mc = -max_rows(mx) * cs;
#pragma unroll
      for (int j = 0; j < 12; j++) v[j] = __builtin_amdgcn_exp2f(__builtin_fmaf(v[j], cs, mc));
      const float sum = sum_rows(((v[0] + v[1]) + (v[2] + v[3])) + ((v[4] + v[5]) + (v[6] + v[7])) + ((v[8] + v[9]) + (v[10] + v[11])));
      const float inv = __builtin_amdgcn_rcpf(sum);
      pt = (u32x4){pk2(v[0] * inv, v[1] * inv), pk2(v[2] * inv, v[3] * inv), pk2(v[4] * inv, v[5] * inv), pk2(v[6] * inv, v[7] * inv)};
      pm = (u32x2){pk2(v[8] * inv, v[9] * inv), pk2(v[10] * inv, v[11] * inv)};
    }
    if (CLS >= 3) DRAIN();
    BAR_LDS();
    if (CLS >= 3) {
      if (CLS == 4) {
        if (lane == 0) st4c(s, r, OFF_CTRL + (C_PCH + h) * 128, 0);
        if ((s & 7) == 0) STAMP(7, c.role, 16 + s);
        if (s > 0 && lane < 40) {
          const unsigned pb = comm_base(comm, pol, s - 1);
          st16c(MARK, r, so0, pb); st16c(MARK, r, so0, pb + CM_REP);
        }
      }
      if (CLS == 3) {
        const unsigned sb = OFF_PT + (unsigned)((s * NH + h) * 2 + wave - 5) * PTT;
        st16c(pt, r, so0, sb);
        if (g >= 2) st8c(pm, r, so1, sb);
      }
      if (!early && s + 1 < NSEG) {
        pr.need(c, s + 1);
#pragma unroll
        for (int i = 0; i < NT; i++) t[i] = ld16c(r, to + i * 1024u, OFF_QKT + (unsigned)((s + 1) * NH + h) * 16u * KB);
      }
#pragma unroll
      for (int i = 0; i < NT; i++) *(u32x4*)(c.lds + 18432 + ((s + 1) & 1) * 16384 + to + i * 1024) = t[i];
    }
    FSAVE(s);
  }
}
DEV void role_S(const Ctx& c, int h, unsigned comm) {
  u32x4 w[40];
  load_weights<0>(c, w, c.p.Wq + h * 128, 4096, 128 * 4096, c.p.Wk + h * 128, 4096, 128 * 4096, -1);
  STAMP(5, c.role, 6);
  const unsigned pol = ring_ready(c);
  if (c.wave == 0) role_S_loop<0>(c, w, h, comm, pol);
  else if (c.wave < 4) role_S_loop<1>(c, w, h, comm, pol);
  else if (c.wave == 4) role_S_loop<2>(c, w, h, comm, pol);
  else if (c.wave < 7) role_S_loop<3>(c, w, h, comm, pol);
  else role_S_loop<4>(c, w, h, comm, pol);
  BAR_ALL();
  if (c.wave == 7 && c.lane == 0) st4c(NSEG, c.r, OFF_CTRL + (C_PCH + h) * 128, 0);
  FDUMP();
}

DEV void role_V(const Ctx& c, int h, unsigned comm) {
  const int lane = c.lane, wave = c.wave, n = lane & 15, g = lane >> 4;
  const rsrc_t r = c.r;
  u32x4 w[40];
  load_weights<2>(c, w, c.p.Wv + h * 128, 4096, 128 * 4096, c.p.Wo + (size_t)h * 128 * DM, DM, 128, -1);
  STAMP(5, c.role, 6);
  const unsigned pol = ring_ready(c);
  const int delay = TUNEV(3, 2250), gap = TUNEV(5, 2);
  const int idx = (lane & 1) ? 0xEEEE : 0x4444;
  const unsigned ga0 = comm_off(CM_M + wave * 2048u + lane * 16u);
  unsigned char* const gd = c.lds + wave * 2048 + lane * 16;
  const unsigned char* const asrc = c.lds + (2 * g + (lane & 1)) * 128 + (n >> 1) * 16;
  unsigned char* const vd = c.lds + 10240 + (wave >> 2) * 1024 + (4 * g + ((n >> 1) & 1) + 16 * (wave & 3)) * 16 + (2 * (n >> 2) + (n & 1)) * 2;
  const unsigned z0o = comm_off(CM_Z + (unsigned)(8 * g + wave) * 8192u + h * 256u + n * 16u), z1o = comm_off(CM_Z + (unsigned)(32 + wave) * 8192u + h * 256u + n * 16u + g * 4u);
  const unsigned k0o = (unsigned)(8 * g + wave) * 256u + n * 16u, k1o = (unsigned)(32 + wave) * 256u + n * 16u + g * 4u;
  unsigned cgo = now(), z4 = 0;
  u32x4 zz = {0, 0, 0, 0};
  for (int s = 0; s < NSEG; s++) {
    FDECL();
    unsigned base = comm_base(comm, pol, s);
    asm volatile("" : "+s"(base));
    if (wave == 7) Clock::spin_cyc(cgo);
    FSTAMP(2);
    BAR_LDS();
    Looks lk;
    if (wave < 5) lk = gather2(c, base, ga0, gd, gap);
    FSTAMP(0);
    BAR_LDS();
    f32x4 d0 = {0, 0, 0, 0}, d1 = {0, 0, 0, 0};
    {
      u32x4 a[10];
#pragma unroll
      for (int ks = 0; ks < 10; ks++) a[ks] = *(const u32x4*)(asrc + ks * 1024);
      SCHED();
#pragma unroll
      for (int ks = 0; ks < 10; ks += 2) {
        d0 = mfma8(a[ks], w[2 * ks], w[2 * ks + 1], d0, idx);
        d1 = mfma8(a[ks + 1], w[2 * ks + 2], w[2 * ks + 3], d1, idx);
      }
      SCHED();
      lk.retire();
      DRAIN();
      if (s > 0) {
        st16c(zz, r, k0o, OFF_ZM + (unsigned)((s - 1) * NH + h) * 10240u);
        st4c(z4, r, k1o, OFF_ZM + (unsigned)((s - 1) * NH + h) * 10240u);
      }
    }
    d0 += d1;
    const unsigned vv = pk2(d0[0] + d0[1], d0[2] + d0[3]);
    *(unsigned short*)vd = (unsigned short)vv;
    *(unsigned short*)(vd + 32) = (unsigned short)(vv >> 16);
    FSTAMP(1);
    BAR_LDS();
    const u32x4 a0 = *(const u32x4*)(c.lds + 10240 + lane * 16), a1 = *(const u32x4*)(c.lds + 11264 + lane * 16);
    f32x4 y[5];
#pragma unroll
    for (int t = 0; t < 5; t++) y[t] = mfma8(a0, w[20 + 4 * t], w[21 + 4 * t], (f32x4){0, 0, 0, 0}, idx);
#pragma unroll
    for (int t = 0; t < 5; t++) y[t] = mfma8(a1, w[22 + 4 * t], w[23 + 4 * t], y[t], idx);
    unsigned z[5];
#pragma unroll
    for (int t = 0; t < 5; t++) z[t] = nosent(pk2(y[t][0] + y[t][1], y[t][2] + y[t][3]));
    const auto q0 = __builtin_amdgcn_permlane32_swap(z[0], z[2], false, false), q1 = __builtin_amdgcn_permlane32_swap(z[1], z[3], false, false);
    const auto q2 = __builtin_amdgcn_permlane16_swap(q0[0], q1[0], false, false), q3 = __builtin_amdgcn_permlane16_swap(q0[1], q1[1], false, false);
    zz = (u32x4){q2[0], q2[1], q3[0], q3[1]}; z4 = z[4];
    st16c(zz, r, z0o, base);
    st4c(z[4], r, z1o, base);
    FSTAMP(3);
    BAR_LDS();
    if (wave == 7) {
      cgo = now() + delay;
      if (lane == 0 && s > 0) st4c(s - 1, r, OFF_CTRL + (C_PCH + 32 + h) * 128, 0);
    }
    FSAVE(s);
  }
  st16c(zz, r, k0o, OFF_ZM + (unsigned)((NSEG - 1) * NH + h) * 10240u);
  st4c(z4, r, k1o, OFF_ZM + (unsigned)((NSEG - 1) * NH + h) * 10240u);
  BAR_ALL();
  if (wave == 7 && lane == 0) st4c(NSEG, r, OFF_CTRL + (C_PCH + 32 + h) * 128, 0);
  FDUMP();
}

template <int CLS>
DEV void role_X_loop(const Ctx& c, int T, unsigned comm, unsigned pol) {
  const int lane = c.lane, wave = c.wave, n = lane & 15, g = lane >> 4;
  const rsrc_t r = c.r;
  Prog pr; pr.init(C_PVZ + 4 * wave, 4, lane);
  const int margin = TUNEV(2, 2900), lfast = TUNEV(4, 1150), lslow = TUNEV(6, 1550), win = TUNEV(7, 250);
  const int idx = (lane & 1) ? 0xEEEE : 0x4444;
  const unsigned po = (unsigned)(lane & 31) * 16u;
  const unsigned pa0 = comm_off(CM_P + (4 * wave + (g >> 1)) * 640u + po), pa1 = comm_off(CM_P + (4 * wave + 2 + (g >> 1)) * 640u + po);
  const unsigned pb = comm_off(CM_P + (4 * wave + g) * 640u + 512u + (n >> 1) * 16u);
  const unsigned za = comm_off(CM_Z + T * 8192u + wave * 1024u + lane * 16u);
  const unsigned mo = comm_off(CM_M + T * 256u + lane * 4u);
  float* const rw = (float*)c.lds + wave * 128 + g * 32 + n;
  const float* const rr = (const float*)c.lds + ((lane >> 2) & 7) * 16 + (lane & 3) * 4 + (lane >> 5) * 2;
  unsigned tp = now() - margin;
  for (int s = 0; s + 1 < NSEG; s++) {
    FDECL();
    pr.need(c, s);
    unsigned base = comm_base(comm, pol, s), nbase = comm_base(comm, pol, s + 1), v = 0;
    asm volatile("" : "+s"(base), "+s"(nbase));
    if (CLS == 0) v = ld4c(r, pace_addr(comm, pol, s), 0);
    BAR_LDS();
    u32x4 zt[4];
#pragma unroll
    for (int i = 0; i < 4; i++) zt[i] = ld16c(r, lane * 16u, OFF_ZT + (unsigned)((s * NH + 4 * wave + i) * 40 + T) * KB);
    if (CLS == 0) pr.v = ld4c(r, pr.addr, 0); else pr.refresh(c, s + 1);
    if ((s & 7) == 0) STAMP(7, c.role, 16 + s);
    {
      const unsigned pb_ = comm_base(comm, pol, s + 3);
      st16c(MARK, r, za, pb_);
      if (CLS == 1 && wave == 1 && lane < 48) st16c(MARK, r, comm_off(CM_M + T * 256u + n * 16u) + (g == 2 ? CM_REP3 : g * CM_REP), pb_);
    }
    if (CLS == 0) {
      v = __builtin_amdgcn_readfirstlane(v);
      GSTAMP(s, 0);
      Clock::spin_cyc(tp + margin);
    }
    FSTAMP(1);
    BAR_LDS();
    u32x4 p0 = ld16c(r, pa0, base), p1 = ld16c(r, pa1, base), pm = ld16c(r, pb, base), zm = ld16c(r, za, base);
    if (__builtin_expect(__builtin_amdgcn_ballot_w64(umax(umax(max4(zm), max4(p0)), umax(max4(p1), max4(pm))) == SENT) != 0, 0)) {
#pragma unroll 1
      do {
        FENCE();
        zm = ld16c(r, za, base); p0 = ld16c(r, pa0, base + CM_REP); p1 = ld16c(r, pa1, base + CM_REP); pm = ld16c(r, pb, base + CM_REP);
      } while (any_unwritten(umax(umax(max4(zm), max4(p0)), umax(max4(p1), max4(pm))), c.budget));
    }
    DRAIN();
    f32x4 acc = mfma8(p0, zt[0], zt[1], (f32x4){0, 0, 0, 0}, idx);
    const f32x4 am = mfma16(pm, zm, (f32x4){0, 0, 0, 0});
    acc = mfma8(p1, zt[2], zt[3], acc, idx);
    rw[0] = (acc[0] + acc[1]) + am[0];
    rw[16] = (acc[2] + acc[3]) + am[2];
    FSTAMP(2);
    BAR_LDS();
    if (CLS == 0) {
      f32x2 u[8];
#pragma unroll
      for (int i = 0; i < 8; i++) u[i] = *(const f32x2*)(rr + i * 128);
      const f32x2 tt = ((u[0] + u[1]) + (u[2] + u[3])) + ((u[4] + u[5]) + (u[6] + u[7]));
      const unsigned o = nosent(pk2(tt[0], tt[1]));
      st4c(o, r, mo, nbase);
      FSTAMP(3);
      st4c(o, r, mo, nbase + CM_REP);
      st4c(o, r, mo, nbase + CM_REP3);
      tp = now();
    }
    FSAVE(s);
  }
}
DEV int xcol(int cg, int c) { return 16 * (cg >> 1) + 4 * (c >> 1) + 2 * (cg & 1) + (c & 1); }
DEV void role_X(const Ctx& c, int T, unsigned comm) {
  const int tid = c.wave * 64 + c.lane;
  if (T < 2) {
    const unsigned cal = comm + (unsigned)T * 2u * MB + 126u * 16384u;
    if (c.wave < 2) st4c(1u, c.r, tid * 128u, cal);
    BAR_ALL();
    unsigned* sl = (unsigned*)c.lds;
    if (c.wave >= 4) {
      unsigned b0 = 0xFFFFFFFFu, b1 = 0xFFFFFFFFu, acc = 0;
#pragma unroll 1
      for (int i = 0; i < 12; i++) {
        const unsigned t0 = now();
        acc += ld4c(c.r, ((i ^ c.wave) & 1) * 8192u + ((c.wave - 4) * 16 + (i >> 1) * 2) * 128u, cal);
        asm volatile("" : "+v"(acc));
        const unsigned d = now() - t0;
        if ((i ^ c.wave) & 1) b1 = d < b1 ? d : b1; else b0 = d < b0 ? d : b0;
      }
      if (c.lane == 0) { sl[c.wave] = b0; sl[8 + c.wave] = b1; }
    }
    BAR_LDS();
    if (tid == 0) {
      const unsigned b0 = min(min(sl[4], sl[5]), min(sl[6], sl[7])), b1 = min(min(sl[12], sl[13]), min(sl[14], sl[15]));
      unsigned pol = (b1 < b0 ? 1u : 0u) ^ (xcc_id() >> 2);
      st4c(pol + 1u, c.r, OFF_CTRL + (C_POL + T) * 128, 0);
    }
    BAR_LDS();
  } else {
    for (int u = (T - 2) * 512 + tid; u < 202 * 1024; u += (NX - 2) * 512) {
      const int blk = u >> 10, pg = blk >= 102;
      u32x4 val = MARK;
      const unsigned lo = ((unsigned)blk & 3u) * 8192u + ((unsigned)u & 511u) * 16u;
      if ((blk < 8 || blk == 48 || blk == 49) && lo < 10240u) {
        const int q = lo >> 4;
        const unsigned* m = (const unsigned*)(c.p.mem0 + (q & 7) * DM + xcol(q >> 3, 0));
        val = (u32x4){nosent(m[0]), nosent(m[2]), nosent(m[4]), nosent(m[6])};
      }
      st16c(val, c.r, (unsigned)pg * 2u * MB + (unsigned)(u - pg * 102 * 1024) * 16u, comm);
    }
    BAR_ALL();
    if (tid == 0) atomic_inc(c.p.ws, C_FILL);
    u32x4 v[9];
#pragma unroll
    for (int i = 0; i < 9; i++) { const int u = (i * (NX - 2) + T - 2) * 512 + tid; v[i] = *(const u32x4*)(c.p.x + (size_t)(u < 163840 ? u : 0) * 8); }
#pragma unroll
    for (int i = 0; i < 9; i++) asm volatile("" ::"v"(v[i]));
  }
  STAMP(5, c.role, 6);
  const unsigned pol = ring_ready(c);
  if (c.wave == 0) role_X_loop<0>(c, T, comm, pol);
  else role_X_loop<1>(c, T, comm, pol);
  FDUMP();
}

template <int N, int D, class A, class F>
DEV void piped(A addr, F use) {
  u32x4 b[D];
#pragma unroll
  for (int i = 0; i < D; i++) b[i] = *(const u32x4*)addr(i);
  SCHED();
#pragma unroll
  for (int i = 0; i < N; i++) {
    use(i, b[i % D]);
    if (i + D < N) b[i % D] = *(const u32x4*)addr(i + D);
    SCHED();
  }
}

template <int HALF>
DEV void role_QK_loop(const Ctx& c, const u32x4 (&w)[40], int h) {
  const int lane = c.lane, wave = c.wave;
  for (int s = 0; s < NSEG; s++) {
    const int nx = s + 1 < NSEG ? s + 1 : s;
    if (HALF == 0) stage_x(c, nx, (s + 1) & 1);
    const XTile xb(c, s & 1);
    const unsigned b = OFF_QKT + (unsigned)(s * NH + h) * 16u * KB + HALF * 8192u + (wave & 3) * 1024u;
    f32x4 d[2][2] = {};
    piped<40, 8>([&](int i) { return xb.at(i / 20, i % 20); }, [&](int i, const u32x4& xf) {
      d[i / 20][0] = mfma16(w[i % 20], xf, d[i / 20][0]);
      d[i / 20][1] = mfma16(w[20 + i % 20], xf, d[i / 20][1]);
      if (HALF == 1 && i == 12) stage_x(c, nx, (s + 1) & 1);
    });
#pragma unroll
    for (int rt = 0; rt < 2; rt++)
      st16c((u32x4){pk2(d[rt][0][0], d[rt][1][0]), pk2(d[rt][0][1], d[rt][1][1]), pk2(d[rt][0][2], d[rt][1][2]), pk2(d[rt][0][3], d[rt][1][3])}, c.r, lane * 16u, b + rt * 4096u);
    if (s < 6) BAR_ALL(); else WAIT_BAR(2);
    if (wave == 0 && lane == 0) st4c(s < 6 ? s + 1 : s, c.r, OFF_CTRL + (C_PQK + h) * 128, 0);
    STAMP(5, c.role, 16 + s);
  }
}
DEV void role_QK(const Ctx& c, int h) {
  u32x4 w[40];
  load_weights<0>(c, w, c.p.Wq + h * 128, 4096, 128 * 4096, c.p.Wk + h * 128, 4096, 128 * 4096, 0);
  STAMP(5, c.role, 6);
  BAR_ALL();
  if (c.wave < 4) role_QK_loop<0>(c, w, h); else role_QK_loop<1>(c, w, h);
  BAR_ALL();
  if (c.wave == 0 && c.lane == 0) st4c(NSEG, c.r, OFF_CTRL + (C_PQK + h) * 128, 0);
}

template <int HALF>
DEV void role_VZ_loop(const Ctx& c, const u32x4 (&w)[40], int h) {
  const int lane = c.lane, wave = c.wave, n = lane & 15, g = lane >> 4;
  const unsigned vo = 40960 + ((n >> 1) & 1) * 4096 + (wave >> 1) * 1024 + (8 * (n >> 3) + 2 * ((n >> 2) & 1) + (n & 1) + 16 * (2 * (wave & 1) + (g >> 1))) * 16 + (g & 1) * 8;
  auto stage1 = [&](int s) {
    const XTile xb(c, s & 1);
    f32x4 d0 = {0, 0, 0, 0}, d1 = {0, 0, 0, 0};
    piped<40, 10>([&](int i) { return xb.at(i & 1, i >> 1); }, [&](int i, const u32x4& xf) {
      if (i & 1) d1 = mfma16(w[i >> 1], xf, d1); else d0 = mfma16(w[i >> 1], xf, d0);
    });
    unsigned char* dst = c.lds + vo + (s & 1) * 8192;
    *(u32x2*)dst = (u32x2){pk2(d0[0], d0[1]), pk2(d0[2], d0[3])};
    *(u32x2*)(dst + 64) = (u32x2){pk2(d1[0], d1[1]), pk2(d1[2], d1[3])};
  };
  auto stage2 = [&](int s) {
    u32x4 va[8];
#pragma unroll
    for (int i = 0; i < 8; i++) va[i] = *(const u32x4*)(c.lds + 40960 + (s & 1) * 8192 + i * 1024 + lane * 16);
    SCHED();
    const unsigned b = OFF_ZT + (unsigned)((s * NH + h) * 40 + wave) * KB;
    f32x4 z[5][2] = {};
#pragma unroll
    for (int t = 0; t < 5; t++) {
#pragma unroll
      for (int ke = 0; ke < 4; ke++) {
        z[t][0] = mfma16(va[ke], w[20 + t * 4 + ke], z[t][0]);
        z[t][1] = mfma16(va[4 + ke], w[20 + t * 4 + ke], z[t][1]);
      }
      if (t > 0) st16c((u32x4){pk2(z[t - 1][0][0], z[t - 1][0][1]), pk2(z[t - 1][0][2], z[t - 1][0][3]), pk2(z[t - 1][1][0], z[t - 1][1][1]), pk2(z[t - 1][1][2], z[t - 1][1][3])}, c.r, lane * 16u, b + (t - 1) * 8192u);
      SCHED();
    }
    st16c((u32x4){pk2(z[4][0][0], z[4][0][1]), pk2(z[4][0][2], z[4][0][3]), pk2(z[4][1][0], z[4][1][1]), pk2(z[4][1][2], z[4][1][3])}, c.r, lane * 16u, b + 4 * 8192u);
  };
  stage_x(c, 1, 1);
  stage1(0);
  BAR_ALL();
  for (int s = 1; s < NSEG; s++) {
    const int nx = s + 1 < NSEG ? s + 1 : s;
    if (HALF == 0) { stage_x(c, nx, (s + 1) & 1); stage1(s); stage2(s - 1); }
    else { stage2(s - 1); stage_x(c, nx, (s + 1) & 1); stage1(s); }
    if (s < 8 || HALF == 1) BAR_ALL(); else WAIT_BAR(5);
    if (wave == 0 && lane == 0) st4c(s < 8 ? s : s - 1, c.r, OFF_CTRL + (C_PVZ + h) * 128, 0);
    STAMP(5, c.role, 16 + s - 1);
  }
  stage2(NSEG - 1);
}
DEV void role_VZ(const Ctx& c, int h) {
  u32x4 w[40];
  load_weights<2>(c, w, c.p.Wv + h * 128, 4096, 128 * 4096, c.p.Wo + (size_t)h * 128 * DM, DM, 128, 0);
  STAMP(5, c.role, 6);
  BAR_ALL();
  if (c.wave < 4) role_VZ_loop<0>(c, w, h); else role_VZ_loop<1>(c, w, h);
  BAR_ALL();
  if (c.wave == 0 && c.lane == 0) st4c(NSEG, c.r, OFF_CTRL + (C_PVZ + h) * 128, 0);
}

constexpr float CSA = 0.22360679774997896f * 1.4426950408889634f;
DEV void role_qkv(const Ctx& c, int a) {
  const int lane = c.lane, wave = c.wave, n = lane & 15, g = lane >> 4;
  const rsrc_t r = c.r;
  const int cgp = a & 7, s0 = (a >> 3) * 6, s1 = s0 + 6 < NSEG ? s0 + 6 : NSEG;
  const int sl = cgp * 8 + wave;
  u32x4 w[40];
  load_weights<0>(c, w, c.p.wqkv + cgp * 256, 1920, 128 * 1920, c.p.wqkv + cgp * 256 + (cgp < 7 ? 128 : 0), 1920, 128 * 1920, s0, cgp >= 5);
  if (sl == 60) {
#pragma unroll
    for (int ks = 0; ks < 20; ks++) w[ks] = *(const u32x4*)(c.p.gw + ks * 32 + g * 8);
  }
  BAR_ALL();
  const int c8 = (sl < 20 ? sl : sl - 20) * 4 + g, grp5 = c8 % 5, hq = (c8 / 5) * 2 + (grp5 > 2);
  const unsigned tile = sl < 20 ? QAT : KVT, qkb = (sl < 20 ? OFF_QA : OFF_KV) + (unsigned)hq * 64u * tile;
  for (int s = s0; s < s1; s++) {
    if (s + 1 < s1) stage_x(c, s + 1, (s + 1) & 1);
    const XTile xb(c, s & 1);
    if (sl < 40 || sl == 60) {
      f32x4 d[2][2] = {};
      piped<40, 8>([&](int i) { return xb.at(i / 20, i % 20); }, [&](int i, const u32x4& xf) {
        d[i / 20][0] = mfma16(w[i % 20], xf, d[i / 20][0]);
        d[i / 20][1] = mfma16(w[20 + i % 20], xf, d[i / 20][1]);
      });
      if (sl == 60) {
        const float z = rbf(g & 1 ? d[1][0][0] : d[0][0][0]);
        if (g < 2) st4c(__float_as_uint(rbf(1.f / (1.f + __expf(-z)))), r, OFF_GATE + (unsigned)(s * 32 + lane) * 4u, 0);
      } else {
        const float sc = sl < 20 ? CSA : 1.f;
#pragma unroll
        for (int rt = 0; rt < 2; rt++) {
          const int row = sl < 20 ? rt * 16 + n : 16 * (n >> 3) + 8 * rt + (n & 7);
          const u32x4 v = {pk2(d[rt][0][0] * sc, d[rt][0][1] * sc), pk2(d[rt][0][2] * sc, d[rt][0][3] * sc), pk2(d[rt][1][0] * sc, d[rt][1][1] * sc), pk2(d[rt][1][2] * sc, d[rt][1][3] * sc)};
          if (grp5 != 2) st16c(v, r, qkb + (unsigned)(row + 32 * (grp5 == 1 || grp5 == 4)) * 16u, (unsigned)s * tile);
          else {
            st8c((u32x2){v.x, v.y}, r, qkb + 1024u + row * 8u, (unsigned)s * tile);
            st8c((u32x2){v.z, v.w}, r, qkb + 64u * tile + 1024u + row * 8u, (unsigned)s * tile);
          }
        }
      }
    } else if (sl < 60) {
      f32x4 d[2][2] = {};
      piped<40, 8>([&](int i) { return xb.at(i / 20, i % 20); }, [&](int i, const u32x4& xf) {
        d[i / 20][0] = mfma16(xf, w[i % 20], d[i / 20][0]);
        d[i / 20][1] = mfma16(xf, w[20 + i % 20], d[i / 20][1]);
      });
#pragma unroll
      for (int t = 0; t < 2; t++) {
        const int cc = (sl - 40) * 32 + t * 16 + n, hh = cc / 20, dd = cc - hh * 20;
        st16c((u32x4){pk2(d[0][t][0], d[0][t][1]), pk2(d[0][t][2], d[0][t][3]), pk2(d[1][t][0], d[1][t][1]), pk2(d[1][t][2], d[1][t][3])}, r,
              OFF_KV + (unsigned)(hh * 64) * KVT + 1280u + (unsigned)(g * 384 + dd * 16), (unsigned)s * KVT);
      }
    } else if (sl >= 62) {
      const int u = (sl - 62) * 64 + lane;
      st16c((u32x4){0x3F803F80u, 0x3F803F80u, 0x3F803F80u, 0x3F803F80u}, r, OFF_KV + (unsigned)((u >> 2) * 64 + s) * KVT + 1280u + (unsigned)(u & 3) * 384u + 320u, 0);
    }
    BAR_ALL();
  }
  for (int f = a * 8 + wave; f < 800; f += NA * 8) {
    const unsigned short* q = (const unsigned short*)(c.p.wo + (size_t)((f % 20) * 32 + g * 8) * DM + (f / 20) * 16 + n);
    unsigned v[8];
#pragma unroll
    for (int j = 0; j < 8; j++) v[j] = q[j * DM];
    st16c((u32x4){v[0] | (v[1] << 16), v[2] | (v[3] << 16), v[4] | (v[5] << 16), v[6] | (v[7] << 16)}, r, lane * 16u, OFF_WO + (unsigned)f * KB);
  }
  BAR_ALL();
  if (wave == 0 && lane == 0) atomic_inc(c.p.ws, C_QKVDONE);
}

DEV int take_ticket(const Ctx& c, int line) {
  unsigned* slot = (unsigned*)(c.lds + 160 * 1024 - 16);
  BAR_LDS();
  if (c.wave == 0 && c.lane == 0) *slot = atomic_inc(c.p.ws, line);
  BAR_ALL();
  return __builtin_amdgcn_readfirstlane(*slot);
}

DEV f32x16 qk32(u32x4 k0, u32x2 k1, u32x4 q0, u32x2 q1) {
  f32x16 z;
#pragma unroll
  for (int i = 0; i < 16; i++) z[i] = 0;
  z = __builtin_amdgcn_mfma_f32_32x32x16_bf16(as8(k0), as8(q0), z, 0, 0, 0);
  return __builtin_amdgcn_mfma_f32_32x32x8bf16_1k(__builtin_bit_cast(s16x4, k1), __builtin_bit_cast(s16x4, q1), z, 0, 0, 0);
}
DEV u32x2 split_neg(float m) {
  const float h1 = rbf(m), h2 = rbf(m - h1), h3 = rbf((m - h1) - h2);
  return (u32x2){pk2(-h1, -h2), pk2(-h3, 0.f)};
}
DEV float max16(const f32x16& s) {
  float mx = s[0];
#pragma unroll
  for (int i = 1; i < 16; i++) mx = __builtin_fmaxf(mx, s[i]);
  return max_halves(mx);
}
DEV u32x4 exp8(const f32x16& s, int o, float sh) {
  float e[8];
#pragma unroll
  for (int j = 0; j < 8; j++) e[j] = __builtin_amdgcn_exp2f(s[o + j] - sh);
  return (u32x4){pk2(e[0], e[1]), pk2(e[2], e[3]), pk2(e[4], e[5]), pk2(e[6], e[7])};
}
DEV f32x16 pv32(u32x4 v, u32x4 p, const f32x16& o) { return __builtin_amdgcn_mfma_f32_32x32x16_bf16(as8(v), as8(p), o, 0, 0, 0); }

DEV void attn_unit(const Ctx& c, int h, int qb) {
  const int lane = c.lane, wave = c.wave, tid = wave * 64 + lane, q = lane & 31, hf = lane >> 5;
  const rsrc_t r = c.r;
  constexpr int CH = 8 * KVT;
  const unsigned qo = OFF_QA + (unsigned)(h * 64 + qb * 8 + wave) * QAT;
  const u32x4 q0 = ld16c(r, lane * 16u, qo);
  u32x2 q1 = ld8c(r, 1024u + q * 8u, qo);
  if (hf) q1 = (u32x2){0, 0};
  const unsigned kvo = OFF_KV + (unsigned)(h * 64) * KVT;
  const bool third = tid < (CH / 16 - 1024);
  u32x4 st0 = ld16c(r, tid * 16u, kvo), st1 = ld16c(r, tid * 16u + 8192u, kvo), st2 = ld16c(r, (third ? tid : 0) * 16u + 16384u, kvo);
  *(u32x4*)(c.lds + tid * 16) = st0; *(u32x4*)(c.lds + tid * 16 + 8192) = st1;
  if (third) *(u32x4*)(c.lds + tid * 16 + 16384) = st2;
  BAR_LDS();
  const int k1o = hf ? 1280 + 320 : 1024 + q * 8, vo = 1280 + hf * 384 + (q < 20 ? q : 20) * 16;
  float m = max16(qk32(*(const u32x4*)(c.lds + lane * 16), *(const u32x2*)(c.lds + k1o), q0, q1));
  if (hf) q1 = split_neg(m);
  f32x16 o;
#pragma unroll
  for (int i = 0; i < 16; i++) o[i] = 0;
  for (int ch = 0; ch < 8; ch++) {
    if (ch + 1 < 8) {
      const unsigned b = kvo + (unsigned)(ch + 1) * CH;
      st0 = ld16c(r, tid * 16u, b); st1 = ld16c(r, tid * 16u + 8192u, b); st2 = ld16c(r, (third ? tid : 0) * 16u + 16384u, b);
    }
    const unsigned char* kb = c.lds + (ch & 1) * CH;
    const f32x16 keep = o;
    {
      u32x4 k0 = *(const u32x4*)(kb + lane * 16); u32x2 k1 = *(const u32x2*)(kb + k1o);
      f32x16 sc = qk32(k0, k1, q0, q1);
      k0 = *(const u32x4*)(kb + KVT + lane * 16); k1 = *(const u32x2*)(kb + KVT + k1o);
#pragma unroll
      for (int t = 0; t < 8; t++) {
        const u32x4 v0 = *(const u32x4*)(kb + t * KVT + vo), v1 = *(const u32x4*)(kb + t * KVT + vo + 768);
        f32x16 sn;
#pragma unroll
        for (int i = 0; i < 16; i++) sn[i] = 0;
        float e[8];
        SCHED();
#pragma unroll
        for (int j = 0; j < 4; j++) e[j] = __builtin_amdgcn_exp2f(sc[j]);
        SCHED();
        if (t + 1 < 8) sn = __builtin_amdgcn_mfma_f32_32x32x16_bf16(as8(k0), as8(q0), sn, 0, 0, 0);
        SCHED();
#pragma unroll
        for (int j = 4; j < 8; j++) e[j] = __builtin_amdgcn_exp2f(sc[j]);
        const u32x4 pa = {pk2(e[0], e[1]), pk2(e[2], e[3]), pk2(e[4], e[5]), pk2(e[6], e[7])};
        SCHED();
        if (t + 1 < 8) sn = __builtin_amdgcn_mfma_f32_32x32x8bf16_1k(__builtin_bit_cast(s16x4, k1), __builtin_bit_cast(s16x4, q1), sn, 0, 0, 0);
        if (t + 2 < 8) { k0 = *(const u32x4*)(kb + (t + 2) * KVT + lane * 16); k1 = *(const u32x2*)(kb + (t + 2) * KVT + k1o); }
        SCHED();
#pragma unroll
        for (int j = 0; j < 4; j++) e[j] = __builtin_amdgcn_exp2f(sc[8 + j]);
        SCHED();
        o = pv32(v0, pa, o);
        SCHED();
#pragma unroll
        for (int j = 4; j < 8; j++) e[j] = __builtin_amdgcn_exp2f(sc[8 + j]);
        const u32x4 pb = {pk2(e[0], e[1]), pk2(e[2], e[3]), pk2(e[4], e[5]), pk2(e[6], e[7])};
        SCHED();
        o = pv32(v1, pb, o);
        if (t + 1 < 8) sc = sn;
      }
    }
    if (__builtin_amdgcn_ballot_w64(!(o[12] < 1.0e9f)) != 0) {
      o = keep;
#pragma unroll 1
      for (int t = 0; t < 8; t++) {
        const unsigned char* kt = kb + t * KVT;
        const f32x16 sc = qk32(*(const u32x4*)(kt + lane * 16), *(const u32x2*)(kt + k1o), q0, q1);
        const float mn = m + __builtin_fmaxf(max16(sc), 0.f), sh = mn - m, al = __builtin_amdgcn_exp2f(-sh);
#pragma unroll
        for (int i = 0; i < 16; i++) o[i] *= al;
        m = mn;
        if (hf) q1 = split_neg(m);
        o = pv32(*(const u32x4*)(kt + vo), exp8(sc, 0, sh), o);
        o = pv32(*(const u32x4*)(kt + vo + 768), exp8(sc, 8, sh), o);
      }
    }
    if (ch + 1 < 8) {
      unsigned char* nb = c.lds + ((ch + 1) & 1) * CH;
      *(u32x4*)(nb + tid * 16) = st0; *(u32x4*)(nb + tid * 16 + 8192) = st1;
      if (third) *(u32x4*)(nb + tid * 16 + 16384) = st2;
    }
    BAR_LDS();
  }
  const float inv = 1.0f / o[12];
  const unsigned ob = OFF_OF + (unsigned)(qb * 8 + wave) * 40960u, ol = (q >> 4) * 20480u + (q & 15) * 16u;
#pragma unroll
  for (int b = 0; b < 3; b++) {
    const int col = h * 20 + 8 * b + 4 * hf;
    if (b < 2 || !hf)
      st8c((u32x2){pk2(o[4 * b] * inv, o[4 * b + 1] * inv), pk2(o[4 * b + 2] * inv, o[4 * b + 3] * inv)}, r,
           ol + (unsigned)((col >> 5) * 1024 + ((col >> 3) & 3) * 256 + (col & 7) * 2), ob);
  }
  BAR_ALL();
  if (wave == 0 && lane == 0) atomic_inc(c.p.ws, C_AT + qb);
}

DEV void final_unit(const Ctx& c, int seg, int cb) {
  const int lane = c.lane, wave = c.wave, tid = wave * 64 + lane, n = lane & 15, g = lane >> 4;
  const rsrc_t r = c.r;
  const int T = cb * 8 + wave;
  if (wave == 0) { wait_lines(c, C_AT + (seg >> 3), 1, 32, 1); wait_lines(c, C_PVZ, 32, seg + 1, 1); }
  BAR_LDS();
  f32x4 a0 = {0, 0, 0, 0}, a1 = {0, 0, 0, 0};
  u32x4 zt[32];
  {
    u32x4 t[5], wf[20];
#pragma unroll
    for (int i = 0; i < 5; i++) t[i] = ld16c(r, tid * 16u, OFF_OF + (unsigned)seg * 40960u + i * 8192u);
#pragma unroll
    for (int i = 0; i < 20; i++) wf[i] = ld16c(r, lane * 16u + (i & 3) * 1024u, OFF_WO + (unsigned)T * 20480u + (i >> 2) * 4096u);
#pragma unroll
    for (int h = 0; h < 20; h++) zt[h] = ld16c(r, lane * 16u, OFF_ZT + (unsigned)((seg * NH + h) * 40 + T) * KB);
    SCHED();
#pragma unroll
    for (int i = 0; i < 5; i++) *(u32x4*)(c.lds + i * 8192 + tid * 16) = t[i];
    BAR_LDS();
    piped<40, 4>([&](int i) { return c.lds + ((i & 1) * 20 + (i >> 1)) * 1024 + lane * 16; }, [&](int i, const u32x4& of) {
      if (i & 1) a1 = mfma16(wf[i >> 1], of, a1); else a0 = mfma16(wf[i >> 1], of, a0);
    });
    asm volatile("" : "+v"(a0), "+v"(a1));
  }
#pragma unroll
  for (int h = 20; h < 32; h++) zt[h] = ld16c(r, lane * 16u, OFF_ZT + (unsigned)((seg * NH + h) * 40 + T) * KB);
  const float g0 = __uint_as_float(ld4c(r, n * 4u, OFF_GATE + seg * 128u)), g1 = __uint_as_float(ld4c(r, n * 4u + 64u, OFF_GATE + seg * 128u));
  if (wave == 0) { wait_lines(c, C_PCH, 1, seg, 1); wait_lines(c, C_PCH, 64, seg + 1, 0); }
  BAR_LDS();
  unsigned char* pl = c.lds + 40960;
  u32x4 zm[8];
  {
    u32x4 t[10];
#pragma unroll
    for (int i = 0; i < 10; i++) t[i] = ld16c(r, tid * 16u, OFF_PT + (unsigned)seg * 81920u + i * 8192u);
#pragma unroll
    for (int i = 0; i < 8; i++) zm[i] = ld16c(r, g * 10240u + n * 16u, OFF_ZM + (unsigned)(seg * NH + 4 * i) * 10240u + T * 256u);
    SCHED();
#pragma unroll
    for (int i = 0; i < 10; i++) *(u32x4*)(pl + i * 8192 + tid * 16) = t[i];
  }
  BAR_LDS();
  f32x4 b0 = {0, 0, 0, 0}, b1 = {0, 0, 0, 0};
  piped<64, 8>([&](int i) { return pl + i * PTT + lane * 16; }, [&](int i, const u32x4& pf) {
    if (i & 1) b1 = mfma16(zt[i >> 1], pf, b1); else b0 = mfma16(zt[i >> 1], pf, b0);
  });
  piped<16, 8>([&](int i) { return pl + ((4 * (i >> 1) + g) * 2 + (i & 1)) * PTT + 1024 + n * 16; }, [&](int i, const u32x4& pf) {
    if (i & 1) b1 = mfma16(zm[i >> 1], pf, b1); else b0 = mfma16(zm[i >> 1], pf, b0);
  });
  const float h0 = rbf(1.f - g0), h1 = rbf(1.f - g1);
  float y0[4], y1[4];
#pragma unroll
  for (int j = 0; j < 4; j++) {
    y0[j] = rbf(g0 * rbf(a0[j])) + rbf(h0 * rbf(b0[j]));
    y1[j] = rbf(g1 * rbf(a1[j])) + rbf(h1 * rbf(b1[j]));
  }
  bf16* o = c.p.out + (size_t)(seg * 32 + n) * DM + T * 16 + 4 * g;
  *(u32x2*)o = (u32x2){pk2(y0[0], y0[1]), pk2(y0[2], y0[3])};
  *(u32x2*)(o + 16 * DM) = (u32x2){pk2(y1[0], y1[1]), pk2(y1[2], y1[3])};
}

__global__ void __launch_bounds__(512, 2) main_kernel(Params p) {
  extern __shared__ __attribute__((aligned(16))) unsigned char lds[];
  const int lane = threadIdx.x & 63, wave = __builtin_amdgcn_readfirstlane(threadIdx.x >> 6);
  Ctx c{p, make_rsrc(p.ws, WS_USED), lds, lane, wave, 0, SPIN_CAP};
  const unsigned comm = comm_start(p.ws);
  unsigned* sl = (unsigned*)(lds + 160 * 1024 - 128);
  if (wave == 0) {
    const unsigned x = xcc_id();
    unsigned t = 0;
    if (lane == 0) t = atomic_inc(p.ws, C_TICKET + x);
    t = __builtin_amdgcn_readfirstlane(t);
    unsigned role = x * 32 + t;
    if (t >= 32) {
#pragma unroll 1
      for (;;) {
        FENCE();
        unsigned v = ld4c(c.r, OFF_CTRL + (C_TICKET + (lane & 7)) * 128, 0);
        v += __shfl_xor(v, 1); v += __shfl_xor(v, 2); v += __shfl_xor(v, 4);
        if (__builtin_amdgcn_readfirstlane(v) >= 256u || --c.budget < 0) break;
      }
      unsigned k = 0;
      if (lane == 0) k = atomic_inc(p.ws, C_OVF);
      k = __builtin_amdgcn_readfirstlane(k);
      role = 0xFFFFu;
      for (int y = 0; y < 8; y++) {
        const unsigned cnt = __builtin_amdgcn_readfirstlane(ld4c(c.r, OFF_CTRL + (C_TICKET + y) * 128, 0));
        const unsigned vac = cnt < 32 ? 32 - cnt : 0;
        if (role == 0xFFFFu) { if (k < vac) role = y * 32 + cnt + k; else k -= vac; }
      }
    }
    if (lane == 0) sl[0] = role;
  }
  BAR_ALL();
  const int role = __builtin_amdgcn_readfirstlane(sl[0]);
  const int grp = role >> 5, slot = role & 31;
  if (grp > 7) return;
  c.role = role;
  STAMP(5, c.role, 1);
  if (grp < 2) { if (slot < 16) role_S(c, grp * 16 + slot, comm); else role_V(c, grp * 16 + slot - 16, comm); return; }
  if (grp < 4 && slot < 20) { role_X(c, (grp - 2) * 20 + slot, comm); return; }
  if (grp >= 4 && slot < 8) role_QK(c, (grp - 4) * 8 + slot);
  else if (grp >= 4 && slot < 16) role_VZ(c, (grp - 4) * 8 + slot - 8);
  else role_qkv(c, grp < 4 ? 64 + (grp - 2) * 12 + slot - 20 : (grp - 4) * 16 + slot - 16);
  STAMP(5, c.role, 2);
  if (wave == 0) wait_lines(c, C_QKVDONE, 1, NA, 1);
  STAMP(0, c.role, 3);
  for (;;) {
    const int u = take_ticket(c, C_QATT);
    if (u >= 256) break;
    attn_unit(c, u & 31, u >> 5);
    STAMP(5, 256, u);
  }
  STAMP(5, c.role, 4);
  for (;;) {
    const int u = take_ticket(c, C_QFIN);
    if (u >= NSEG * 5) break;
    final_unit(c, u / 5, u % 5);
    STAMP(5, 257, u);
  }
  STAMP(5, c.role, 5);
}

extern "C" void kernel_launch(void* const* d_in, const int* in_sizes, int n_in, void* d_out, int out_size, void* d_ws, size_t ws_size,
                              hipStream_t stream) {
  Params p;
  p.x = (const bf16*)d_in[0]; p.wqkv = (const bf16*)d_in[1]; p.wo = (const bf16*)d_in[2]; p.mem0 = (const bf16*)d_in[4];
  p.Wq = (const bf16*)d_in[5]; p.Wk = (const bf16*)d_in[6]; p.Wv = (const bf16*)d_in[7]; p.Wo = (const bf16*)d_in[8];
  p.gw = (const bf16*)d_in[10]; p.out = (bf16*)d_out; p.ws = (unsigned char*)d_ws;
  (void)hipFuncSetAttribute((const void*)main_kernel, hipFuncAttributeMaxDynamicSharedMemorySize, 160 * 1024);
  (void)hipMemsetAsync(d_ws, 0, C_LINES * 128, stream);
  main_kernel<<<256, 512, 160 * 1024, stream>>>(p);
}
```

```cpp
#include <hip/hip_runtime.h>
#include <hip/hip_bf16.h>

typedef __bf16 bf16;
typedef __attribute__((ext_vector_type(2))) __bf16 bf16x2;
typedef __attribute__((ext_vector_type(8))) __bf16 bf16x8;
typedef __attribute__((ext_vector_type(16))) __bf16 bf16x16;
typedef __attribute__((ext_vector_type(4))) short s16x4;
typedef __attribute__((ext_vector_type(2))) float f32x2;
typedef __attribute__((ext_vector_type(4))) float f32x4;
typedef __attribute__((ext_vector_type(16))) float f32x16;
typedef __attribute__((ext_vector_type(2))) unsigned u32x2;
typedef __attribute__((ext_vector_type(4))) unsigned u32x4;
typedef __attribute__((ext_vector_type(8))) unsigned u32x8;
typedef __amdgpu_buffer_rsrc_t rsrc_t;
#define DEV __device__ __forceinline__
#define SC1 16

#define SSLEEP 2
constexpr int NSEG = 64, NH = 32, DM = 640;
constexpr unsigned KB = 1024u, MB = 1024u * 1024u;
constexpr unsigned OFF_CTRL = 0;
constexpr unsigned OFF_CLAIM = 128 * KB;
constexpr unsigned OFF_GATE = 64 * KB;
constexpr unsigned OFF_WO = 1 * MB;
constexpr unsigned OFF_OF = 2 * MB;
constexpr unsigned OFF_QA = 5 * MB;
constexpr unsigned OFF_KV = 8 * MB;
constexpr unsigned OFF_PT = 14 * MB;
constexpr unsigned OFF_COMM = 20 * MB;
constexpr unsigned OFF_QKT = 26 * MB;
constexpr unsigned OFF_ZM = 58 * MB;
constexpr unsigned OFF_ZT = 78 * MB;
constexpr unsigned OFF_BP = 158 * MB;
constexpr unsigned WS_USED = 161 * MB;
constexpr int C_SEED = 0, C_READY = 1;
constexpr int C_QTAIL = 8, C_QKVDONE = 9, C_QATT = 10, C_QFIN = 11;
constexpr int C_FILL = 12, C_POL = 13;
constexpr int C_AT = 16;
constexpr int C_PQK = 32;
constexpr int C_PVZ = 96;
constexpr int C_PCH = 160;
constexpr int C_QB = 24, C_QA = 25;
constexpr int C_BD = 224;
constexpr int C_LINES = 288;
#define SEG0 48
#define NBX 4
constexpr int S0 = SEG0;
constexpr int NA = 24, NX = 40, QSEG = 22;
#define KHELP 14
constexpr int KH = KHELP;
DEV int seg_set(int s) { return s < 2 * KH ? s & 1 : 0; }
DEV int seg_idx(int s) { return s < 2 * KH ? s >> 1 : s - KH; }
DEV int seg_of(int set, int j) { return set ? 2 * j + 1 : j < KH ? 2 * j : j + KH; }
constexpr unsigned SENT = 0xFFFFFFFFu;
constexpr int SPIN_CAP = 1 << 21;
constexpr int KVT = 2816, QAT = 1280, PTT = 1280;
constexpr unsigned CM_M = 0, CM_P = 10240, CM_Z = 65536, CM_REP = 4 * 16384, CM_REP3 = 48 * 16384  ;
DEV unsigned comm_off(unsigned o) { return ((o >> 13) << 14) | (o & 8191u); }
DEV unsigned comm_base(unsigned comm, unsigned pol, int s) {
  const unsigned pg = ((unsigned)s >> 1) & 1u;
  return comm + pg * 2u * MB + ((unsigned)s & 1u) * 50u * 16384u + (((pol >> pg) & 1u) << 13);
}
DEV unsigned pace_addr(unsigned comm, unsigned pol, int s) { return comm + 100u * 16384u + ((pol & 1u) << 13) + (unsigned)s * 128u; }
#define TUNEV(i, d) (d)

struct Params {
  const bf16 *x, *wqkv, *wo, *mem0, *Wq, *Wk, *Wv, *Wo, *gw;
  bf16* out;
  unsigned char* ws;
};
struct Ctx {
  const Params& p;
  rsrc_t r;
  unsigned char* lds;
  int lane, wave, role;
  mutable int budget;
  unsigned tag;
};

DEV rsrc_t make_rsrc(const void* p, unsigned bytes) { return __builtin_amdgcn_make_buffer_rsrc((void*)p, 0, bytes, 0x00020000); }
DEV unsigned comm_start(const unsigned char* ws) {
  const unsigned lo = (unsigned)((unsigned long long)ws & (2u * MB - 1));
  return OFF_COMM + ((2u * MB - lo) & (2u * MB - 1));
}
DEV unsigned xcc_id() { return (unsigned)__builtin_amdgcn_s_getreg(20 | (3 << 11)) & 7u; }
DEV unsigned pk2(float a, float b) { bf16x2 v = {(bf16)a, (bf16)b}; return __builtin_bit_cast(unsigned, v); }
DEV unsigned nosent(unsigned v) { return v < 0xFFFFFFFEu ? v : 0xFFFFFFFEu; }
DEV float rbf(float a) { return (float)(bf16)a; }
DEV bf16x8 as8(u32x4 v) { return __builtin_bit_cast(bf16x8, v); }
DEV bf16x16 cat16(u32x4 a, u32x4 b) { u32x8 v = {a.x, a.y, a.z, a.w, b.x, b.y, b.z, b.w}; return __builtin_bit_cast(bf16x16, v); }
DEV f32x4 mfma16(u32x4 a, u32x4 b, f32x4 c) { return __builtin_amdgcn_mfma_f32_16x16x32_bf16(as8(a), as8(b), c, 0, 0, 0); }
DEV f32x4 mfma8(u32x4 a, u32x4 b0, u32x4 b1, f32x4 c, int idx) { return __builtin_amdgcn_smfmac_f32_16x16x64_bf16(as8(a), cat16(b0, b1), c, idx, 0, 0); }
DEV f32x2 fold8(f32x4 d) { return (f32x2){d[0], d[1]} + (f32x2){d[2], d[3]}; }
DEV u32x4 ld16c(rsrc_t r, unsigned v, unsigned s) { return __builtin_amdgcn_raw_buffer_load_b128(r, v, s, SC1); }
DEV u32x2 ld8c(rsrc_t r, unsigned v, unsigned s) { return __builtin_amdgcn_raw_buffer_load_b64(r, v, s, SC1); }
DEV unsigned ld4c(rsrc_t r, unsigned v, unsigned s) { return __builtin_amdgcn_raw_buffer_load_b32(r, v, s, SC1); }
DEV void st16c(u32x4 d, rsrc_t r, unsigned v, unsigned s) {
  __builtin_amdgcn_raw_buffer_store_b128(d, r, v, s, SC1);
  asm volatile("s_nop 1" ::"v"(d) : "memory");
}
DEV void st8c(u32x2 d, rsrc_t r, unsigned v, unsigned s) { __builtin_amdgcn_raw_buffer_store_b64(d, r, v, s, SC1); }
DEV void st4c(unsigned d, rsrc_t r, unsigned v, unsigned s) { __builtin_amdgcn_raw_buffer_store_b32(d, r, v, s, SC1); }
DEV unsigned umax(unsigned a, unsigned b) { return a > b ? a : b; }
DEV unsigned max4(u32x4 v) { return umax(umax(umax(v.x, v.y), v.z), v.w); }
DEV bool any_unwritten(unsigned m, int& budget) {
  const unsigned thr = --budget >= 0 ? SENT - 1u : SENT;
  return __builtin_amdgcn_ballot_w64(m > thr) != 0;
}
#define FENCE() asm volatile("" ::: "memory")
#define BAR_LDS() asm volatile("s_waitcnt lgkmcnt(0)\n\ts_barrier" ::: "memory")
#define BAR_ALL() asm volatile("s_waitcnt vmcnt(0) lgkmcnt(0)\n\ts_barrier" ::: "memory")
#define WAIT_BAR(n) asm volatile("s_waitcnt vmcnt(%0) lgkmcnt(0)\n\ts_barrier" ::"n"(n) : "memory")
#define DRAIN() asm volatile("s_waitcnt vmcnt(0)" ::: "memory")
#define SCHED() __builtin_amdgcn_sched_barrier(0)
constexpr u32x4 MARK = {SENT, SENT, SENT, SENT};
DEV float vmax(float a, float b) { float d; asm("v_max_f32 %0, %1, %2" : "=v"(d) : "v"(a), "v"(b)); return d; }
DEV float max_halves(float v) {
  auto b = __builtin_amdgcn_permlane32_swap(__float_as_uint(v), __float_as_uint(v), false, false);
  return vmax(__uint_as_float(b[0]), __uint_as_float(b[1]));
}
DEV float max_rows(float v) {
  auto a = __builtin_amdgcn_permlane16_swap(__float_as_uint(v), __float_as_uint(v), false, false);
  return max_halves(vmax(__uint_as_float(a[0]), __uint_as_float(a[1])));
}
DEV float sum_rows(float v) {
  auto a = __builtin_amdgcn_permlane16_swap(__float_as_uint(v), __float_as_uint(v), false, false);
  v = __uint_as_float(a[0]) + __uint_as_float(a[1]);
  auto b = __builtin_amdgcn_permlane32_swap(__float_as_uint(v), __float_as_uint(v), false, false);
  return __uint_as_float(b[0]) + __uint_as_float(b[1]);
}
DEV float dpp_ror8(float v) { return __uint_as_float(__builtin_amdgcn_update_dpp(0u, __float_as_uint(v), 0x128, 0xf, 0xf, true)); }
DEV unsigned atomic_inc(unsigned char* ws, int line) {
  return __hip_atomic_fetch_add((unsigned*)(ws + OFF_CTRL + line * 128), 1u, __ATOMIC_RELAXED, __HIP_MEMORY_SCOPE_AGENT);
}
DEV unsigned wait_lines(const Ctx& c, int first, int n, unsigned want, int nap) {
  const unsigned a = OFF_CTRL + (unsigned)(first + (c.lane < n ? c.lane : 0)) * 128u;
  unsigned v;
#pragma unroll 1
  for (;;) {
    FENCE();
    v = ld4c(c.r, a, 0);
    if (__builtin_amdgcn_ballot_w64(v < want) == 0 || --c.budget < 0) break;
    if (nap) __builtin_amdgcn_s_sleep(8);
  }
  FENCE();
  return v;
}
DEV void ctrl_ready(const Ctx& c) {
#pragma unroll 1
  for (;;) {
    FENCE();
    if (__builtin_amdgcn_readfirstlane(ld4c(c.r, OFF_CTRL + C_READY * 128, 0)) == c.tag || --c.budget < 0) break;
  }
  FENCE();
}
struct Prog {
  unsigned v, addr;
  DEV void init(int first, int n, int lane) { v = 0; addr = OFF_CTRL + (unsigned)(first + (lane >> 5) * 32 + (lane & (n - 1))) * 128u; }
  DEV bool lacks(int s) const { return (unsigned)(__builtin_amdgcn_ballot_w64(v <= (unsigned)seg_idx(s)) >> (32 * seg_set(s))) != 0u; }
  DEV void need(const Ctx& c, int s) {
    if (lacks(s)) {
      bool again;
#pragma unroll 1
      do {
        FENCE();
        v = ld4c(c.r, addr, 0);
        asm volatile("" : "+v"(v));
        const bool more = --c.budget >= 0;
        again = lacks(s) & more;
      } while (again);
    }
    FENCE();
  }
  DEV void refresh(const Ctx& c, int s) { if (s < NSEG && lacks(s)) v = ld4c(c.r, addr, 0); }
};
DEV unsigned now() { unsigned long long t; asm volatile("s_memtime %0\n\ts_waitcnt lgkmcnt(0)" : "=s"(t)::"memory"); return (unsigned)t; }
struct Clock {
  unsigned r0, c0;
  DEV static void read(unsigned& r, unsigned& cy) {
    unsigned long long a, b;
    asm volatile("s_memrealtime %0\n\ts_memtime %1\n\ts_waitcnt lgkmcnt(0)" : "=&s"(a), "=&s"(b)::"memory");
    r = (unsigned)a * 10u; cy = (unsigned)b;
  }
  DEV void init() { read(r0, c0); r0 += 20u; }
  DEV void align() {
    unsigned r, pr, cy;
    read(pr, cy);
    r = pr;
#pragma unroll
    for (int i = 0; i < 4; i++) if (r == pr) read(r, cy);
    r0 = r + 8u; c0 = cy;
  }
  DEV static unsigned ns(unsigned cycles) { return (cycles * 445u) >> 10; }
  DEV unsigned at(unsigned cy) const { return r0 + ns(cy - c0); }
  DEV unsigned cyc(unsigned t) const {
    const int rem = (int)(t - r0);
    return c0 + (unsigned)(((rem > 4000 ? 4000 : rem < 0 ? 0 : rem) * 2355) >> 10);
  }
  DEV static void spin_cyc(unsigned ct) {
#pragma unroll 1
    for (int i = 0; i < 40; i++)
      if ((int)(now() - ct) >= 0 || (int)(now() - ct) >= 0 || (int)(now() - ct) >= 0 || (int)(now() - ct) >= 0 || (int)(now() - ct) >= 0 || (int)(now() - ct) >= 0) break;
  }
  DEV void spin(unsigned t) const { spin_cyc(cyc(t)); }
  DEV void wait(unsigned t) {
    unsigned r, cy;
    read(r, cy);
    if ((int)(r + 160u - t) < 0) {
#pragma unroll 1
      for (int i = 0; i < 256; i++) {
        const unsigned pr = r;
        read(r, cy);
        if (r == pr) read(r, cy);
        if (r == pr) read(r, cy);
        if (r != pr) { r0 = r + 8u; c0 = cy; }
        if ((int)(r + 200u - t) >= 0) break;
      }
    } else if (cy - c0 > 6000u) { r0 = r + 20u; c0 = cy; }
    spin(t);
  }
};
#define STAMP(w, role, idx) do {} while (0)
#define FDECL() do {} while (0)
#define FSTAMP(k) do {} while (0)
#define FSAVE(s) do {} while (0)
#define FDUMP() do {} while (0)
#define GSTAMP(s, k) do {} while (0)
DEV void glds16(const void* g, unsigned lds_dst) {
  unsigned keep;
  asm volatile("s_mov_b32 %0, m0\n\ts_mov_b32 m0, %2\n\ts_nop 0\n\tglobal_load_lds_dwordx4 %1, off\n\ts_mov_b32 m0, %0"
               : "=&s"(keep) : "v"(g), "s"(lds_dst) : "memory");
}
DEV void glds16w(const void* g, unsigned lds_dst) {
  unsigned keep;
  asm volatile("s_mov_b32 %0, m0\n\ts_mov_b32 m0, %2\n\ts_nop 0\n\tglobal_load_lds_dwordx4 %1, off\n\ts_mov_b32 m0, %0"
               : "=&s"(keep) : "v"(g), "s"(lds_dst) : "memory");
}
DEV unsigned lds_addr(const void* p) { return (unsigned)(unsigned long long)(const __attribute__((address_space(3))) void*)p; }

constexpr unsigned XB0 = 65536, XB1 = 0;
DEV void stage_x(const Ctx& c, int seg, int buf) {
  const bf16* src = c.p.x + (size_t)(seg * 32 + (c.wave >> 1) * 8 + (c.lane >> 3)) * DM + (c.wave & 1) * 320 + ((c.lane & 7) ^ (c.lane >> 3)) * 8;
  const unsigned dst = lds_addr(c.lds) + (buf ? XB1 : XB0) + c.wave * 5120;
#pragma unroll
  for (int i = 0; i < 5; i++) glds16(src + i * 64, dst + i * 1024);
}
struct XTile {
  const unsigned char *e, *o;
  DEV XTile(const Ctx& c, int buf) {
    const int n = c.lane & 15, g = c.lane >> 4;
    const unsigned b = (buf ? XB1 : XB0) + (n >> 3) * 10240 + (n & 7) * 128;
    e = c.lds + b + ((g ^ (n & 7)) << 4);
    o = c.lds + b + (((4 + g) ^ (n & 7)) << 4);
  }
  DEV const unsigned char* at(int rt, int ks) const { return ((ks & 1) ? o : e) + rt * 20480 + (ks >> 1) * 1024; }
};
DEV void dma_block(const Ctx& c, const bf16* src, int ld, unsigned lds_off) {
  const int q = c.lane >> 4;
#pragma unroll
  for (int i = 0; i < 4; i++)
    glds16w(src + (size_t)(c.wave * 16 + i * 4 + q) * ld + (((c.lane & 15) ^ ((q << 2) | i)) << 3), lds_addr(c.lds) + lds_off + (c.wave * 16 + i * 4) * 256);
}
typedef __attribute__((address_space(3))) s16x4* lds_s16x4;
DEV void read_cols(const Ctx& c, unsigned lds_off, int col0, int cstep, u32x4* f) {
  const int g = c.lane >> 4, q = (c.lane >> 2) & 3, col = col0 + cstep * (c.lane & 3);
  const unsigned a0 = lds_addr(c.lds) + lds_off + (8 * g + q) * 256 + (((col >> 3) ^ ((q << 2) | ((2 * g) & 3))) << 4) + ((col >> 2) & 1) * 8;
  const unsigned a1 = lds_addr(c.lds) + lds_off + (8 * g + 4 + q) * 256 + (((col >> 3) ^ ((q << 2) | ((2 * g + 1) & 3))) << 4) + ((col >> 2) & 1) * 8;
#pragma unroll
  for (int ks = 0; ks < 4; ks++) {
    const u32x2 lo = __builtin_bit_cast(u32x2, __builtin_amdgcn_ds_read_tr16_b64_v4i16((lds_s16x4)(size_t)(a0 + ks * 8192)));
    const u32x2 hi = __builtin_bit_cast(u32x2, __builtin_amdgcn_ds_read_tr16_b64_v4i16((lds_s16x4)(size_t)(a1 + ks * 8192)));
    f[ks] = (u32x4){lo.x, lo.y, hi.x, hi.y};
  }
}
template <int MODE>
DEV void load_weights(const Ctx& c, u32x4 (&w)[40], const bf16* A, int lda, int stepa, const bf16* B, int ldb, int stepb, int xseg, bool plain = false) {
#pragma unroll
  for (int j = 0; j < 2; j++) { dma_block(c, A + (size_t)j * stepa, lda, j * 65536); dma_block(c, B + (size_t)j * stepb, ldb, j * 65536 + 32768); }
#pragma unroll
  for (int j = 0; j < 5; j++) {
    if (j < 4) WAIT_BAR(8); else if (xseg >= 0) WAIT_BAR(5); else WAIT_BAR(0);
    const unsigned o = (j & 1) * 65536;
    if (MODE == 2) {
      read_cols(c, o, 16 * c.wave, 4, &w[4 * j]);
      read_cols(c, o + 32768, 16 * c.wave, 4, &w[20 + 4 * j]);
    } else {
      read_cols(c, o + (c.wave >> 2) * 32768, 32 * (c.wave & 3), plain ? 4 : 8, &w[4 * j]);
      read_cols(c, o + (c.wave >> 2) * 32768, 32 * (c.wave & 3) + (plain ? 16 : 4), plain ? 4 : 8, &w[20 + 4 * j]);
    }
    BAR_LDS();
    if (j + 2 < 5) { dma_block(c, A + (size_t)(j + 2) * stepa, lda, o); dma_block(c, B + (size_t)(j + 2) * stepb, ldb, o + 32768); }
    if (j == 3 && xseg >= 0) stage_x(c, xseg, 0);
  }
#pragma unroll
  for (int i = 0; i < 40; i++) asm volatile("" : "+v"(w[i]));
  ctrl_ready(c);
}

DEV unsigned ring_ready(const Ctx& c) {
  wait_lines(c, C_FILL, 1, NX - 2, 0);
  const unsigned v = wait_lines(c, C_POL, 2, 1, 0);
  return (unsigned)__builtin_amdgcn_ballot_w64(c.lane < 2 && v == 2u);
}
DEV bool unwritten2(u32x4 a, u32x4 b) { return __builtin_amdgcn_ballot_w64(umax(max4(a), max4(b)) == SENT) != 0; }
DEV void gather2(const Ctx& c, unsigned base, unsigned a0, unsigned char* dst) {
  u32x4 v0 = ld16c(c.r, a0, base), v1 = ld16c(c.r, a0 + 1024u, base);
  *(u32x4*)dst = v0;
  *(u32x4*)(dst + 1024) = v1;
  SCHED();
  if (__builtin_expect(unwritten2(v0, v1), 0)) {
    unsigned b = base + CM_REP;
#pragma unroll 1
    do {
      FENCE();
      v0 = ld16c(c.r, a0, b); v1 = ld16c(c.r, a0 + 1024u, b);
      b = base + CM_REP3;
    } while (any_unwritten(umax(max4(v0), max4(v1)), c.budget));
    *(u32x4*)dst = v0;
    *(u32x4*)(dst + 1024) = v1;
  }
}
DEV int key_token(int u, int m) { return 16 * ((m >> 2) & 1) + 8 * (m >> 3) + 4 * ((m >> 1) & 1) + 2 * u + (m & 1); }

template <int CLS>
DEV void role_S_loop(const Ctx& c, const u32x4 (&w)[40], int h, unsigned comm, unsigned pol) {
  const int lane = c.lane, wave = c.wave, n = lane & 15, g = lane >> 4;
  const rsrc_t r = c.r;
  const float cs = 0.08838834764831845f * 1.4426950408889634f;
  Prog pr; pr.init(C_PQK + h, 1, lane);
  const int delay = TUNEV(1, 2100);
  const int idx = (lane & 2) ? 0xEEEE : 0x4444;
  const unsigned ga0 = comm_off(CM_M + wave * 2048u + lane * 16u);
  const unsigned so0 = CLS == 0 ? comm_off(CM_P + h * 640u + ((g >> 1) * 16 + 4 * ((n & 7) >> 1) + 2 * (g & 1) + (n & 1)) * 16u + (n >> 3) * 8u) : CLS == 4 ? comm_off(CM_P + h * 640u + lane * 16u) : lane * 16u;
  const unsigned so1 = CLS == 0 ? comm_off(CM_P + h * 640u + 512u + (n & 7) * 16u + (g & 1) * 8u + (g >> 1) * 4u) : 1024u + n * 16u + (g - 2) * 8u;
  constexpr int NT = CLS == 3 ? 5 : 6;
  const unsigned to = (wave - 5) * 5120u + lane * 16u;
  unsigned char* const gd = c.lds + wave * 2048 + lane * 16;
  const unsigned char* const asrc = c.lds + (2 * g + ((n >> 1) & 1)) * 128 + (2 * (n >> 2) + (n & 1)) * 16;
  unsigned char* const xd = c.lds + 10240 + (wave & 3) * 1024 + ((wave >> 2) * 8 + 2 * g + 16 * (n >> 2)) * 16 + (n & 3) * 4;
  const unsigned char* const xq = c.lds + 10240 + ((n & 7) + 16 * g) * 16;
  const unsigned char* const qlo = n < 8 ? xq + wave * 1024 : c.lds + 51200;
  const unsigned char* const qhi = n < 8 ? c.lds + 51200 : xq + wave * 1024;
  unsigned char* const pd = c.lds + 14336 + wave * 1024 + lane * 16;
  if (CLS == 0) *(u32x4*)(c.lds + 51200) = (u32x4){0, 0, 0, 0};
  u32x4 t[NT];
  if (CLS >= 3) {
    pr.need(c, 0);
#pragma unroll
    for (int i = 0; i < NT; i++) t[i] = ld16c(r, to + i * 1024u, OFF_QKT + (unsigned)h * 16u * KB);
#pragma unroll
    for (int i = 0; i < NT; i++) *(u32x4*)(c.lds + 18432 + to + i * 1024) = t[i];
  }
  if (CLS < 2) __builtin_amdgcn_s_setprio(2);
  unsigned cgo = now();
  bool ahead = false;
  unsigned best = 2800;
  const int extra = TUNEV(6, 0);
  for (int s = 0; s < NSEG; s++) {
    FDECL();
    unsigned base = comm_base(comm, pol, s);
    unsigned o_tl = 18432 + (s & 1) * 16384 + (CLS < 2 ? 8192 + wave * 1024 + lane * 16 : 0);
    asm volatile("" : "+s"(base));
    if (CLS < 2) asm volatile("" : "+v"(o_tl));
    const unsigned char* tl = c.lds + o_tl;
    if (CLS == 4) GSTAMP(s, 2);
    if (CLS == 0) Clock::spin_cyc(cgo);
    FSTAMP(2);
    BAR_LDS();
    u32x4 k0, k1;
    if (CLS < 2) { k0 = *(const u32x4*)tl; k1 = *(const u32x4*)(tl + 4096); }
    if (CLS < 3) gather2(c, base, ga0, gd);
    unsigned tb = 0;
    if (CLS >= 3) {
      DRAIN();
      ahead = s + 1 < NSEG && !pr.lacks(s + 1);
      tb = OFF_QKT + (unsigned)((ahead ? s + 1 : s) * NH + h) * 16u * KB;
      asm volatile("" : "+s"(tb));
    }
    if (CLS < 2) asm volatile("" : "+v"(k0), "+v"(k1));
    FSTAMP(0);
    BAR_LDS();
    if (CLS >= 3) {
#pragma unroll
      for (int i = 0; i < NT; i++) t[i] = ld16c(r, to + i * 1024u, tb);
      pr.v = ld4c(r, pr.addr, 0);
      SCHED();
    }
    f32x4 d0 = {0, 0, 0, 0}, d1 = {0, 0, 0, 0};
    {
      u32x4 a[10];
#pragma unroll
      for (int ks = 0; ks < 10; ks++) a[ks] = *(const u32x4*)(asrc + ks * 1024);
      SCHED();
#pragma unroll
      for (int ks = 0; ks < 10; ks++) {
        d0 = mfma8(a[ks], w[2 * ks], w[2 * ks + 1], d0, idx);
        d1 = mfma8(a[ks], w[20 + 2 * ks], w[21 + 2 * ks], d1, idx);
        if (CLS >= 2 && ks == 0) { SCHED(); asm volatile("s_nop 7"); SCHED(); }
      }
      SCHED();
    }
    const f32x2 f0 = fold8(d0), f1 = fold8(d1);
    *(unsigned*)xd = pk2(f0[0], f1[0]);
    *(unsigned*)(xd + 16) = pk2(f0[1], f1[1]);
    if (CLS < 2) {
      FENCE();
      const u32x4 qa = *(const u32x4*)qlo, qb = *(const u32x4*)qhi;
      SCHED();
      *(f32x4*)pd = mfma16(k1, qb, mfma16(k0, qa, (f32x4){0, 0, 0, 0}));
    }
    FSTAMP(1);
    BAR_LDS();
    u32x4 pt; u32x2 pm;
    if (CLS == 0) {
      u32x4 kf[4], qf[4]; f32x4 pp[4];
#pragma unroll
      for (int ke = 0; ke < 4; ke++) { kf[ke] = *(const u32x4*)(xq + 128 + ke * 1024); qf[ke] = *(const u32x4*)(xq + ke * 1024); }
#pragma unroll
      for (int ke = 0; ke < 4; ke++) pp[ke] = *(const f32x4*)(c.lds + 14336 + ke * 1024 + lane * 16);
      SCHED();
      f32x4 sm = mfma16(kf[0], qf[0], (f32x4){0, 0, 0, 0});
#pragma unroll
      for (int ke = 1; ke < 4; ke++) sm = mfma16(kf[ke], qf[ke], sm);
      SCHED();
      const f32x4 st = (pp[0] + pp[1]) + (pp[2] + pp[3]);
      const float ma = n < 8 ? sm[0] : sm[1], mb = n < 8 ? sm[2] : sm[3], mv = g < 2 ? ma : mb;
      float mx = __builtin_fmaxf(__builtin_fmaxf(__builtin_fmaxf(st[0], st[1]), __builtin_fmaxf(st[2], st[3])), mv);
      mx = vmax(mx, dpp_ror8(mx));
      const float mc = -max_rows(mx) * cs;
      const float e0 = __builtin_amdgcn_exp2f(__builtin_fmaf(st[0], cs, mc)), e1 = __builtin_amdgcn_exp2f(__builtin_fmaf(st[1], cs, mc));
      const float e2 = __builtin_amdgcn_exp2f(__builtin_fmaf(st[2], cs, mc)), e3 = __builtin_amdgcn_exp2f(__builtin_fmaf(st[3], cs, mc));
      const float e4 = __builtin_amdgcn_exp2f(__builtin_fmaf(mv, cs, mc));
      const float e5 = dpp_ror8(e4);
      float sum = ((e0 + e1) + (e2 + e3)) + e4;
      sum += dpp_ror8(sum);
      const float inv = __builtin_amdgcn_rcpf(sum_rows(sum));
      u32x2 o0 = {nosent(pk2(e0 * inv, e1 * inv)), nosent(pk2(e2 * inv, e3 * inv))};
      unsigned o1 = nosent(pk2(e4 * inv, e5 * inv));
      asm volatile("" : "+v"(o0), "+v"(o1));
      st8c(o0, r, so0, base);
      if (n < 8) st4c(o1, r, so1, base);
      FSTAMP(3);
      st8c(o0, r, so0, base + CM_REP);
      if (n < 8) st4c(o1, r, so1, base + CM_REP);
      const unsigned cp = now(), took = cp - cgo;
      best = min(best + 4u, took);
      cgo = cp + delay + (took > best + 250u ? extra : 0);
    } else if (CLS == 3) {
      int rel = (wave - 5) * 16 + n - key_token(0, 4 * g);
      asm volatile("" : "+v"(rel));
      const float mneg = g >= 2 ? 0.f : -3.0e38f;
      f32x4 sm = {mneg, mneg, mneg, mneg}, s0, s1;
#pragma unroll
      for (int j = 0; j < 4; j++) { s0[j] = key_token(0, j) <= rel ? 0.f : -3.0e38f; s1[j] = key_token(1, j) <= rel ? 0.f : -3.0e38f; }
      const int kt = key_token(0, n);
      const unsigned char* kp = tl + 8192 + (kt >> 4) * 4096 + ((kt & 15) + 16 * g) * 16;
#pragma unroll
      for (int ke = 0; ke < 4; ke++) {
        const u32x4 qt = *(const u32x4*)(tl + (wave - 5) * 4096 + ke * 1024 + lane * 16);
        s0 = mfma16(*(const u32x4*)(kp + ke * 1024), qt, s0);
        s1 = mfma16(*(const u32x4*)(kp + ke * 1024 + 32), qt, s1);
        sm = mfma16(*(const u32x4*)(c.lds + 10240 + ke * 1024 + lane * 16), qt, sm);
      }
      float v[12];
#pragma unroll
      for (int j = 0; j < 4; j++) { v[j] = s0[j]; v[4 + j] = s1[j]; v[8 + j] = sm[j]; }
      float mx = v[0];
#pragma unroll
      for (int j = 1; j < 12; j++) mx = __builtin_fmaxf(mx, v[j]);
      const float mc = -max_rows(mx) * cs;
#pragma unroll
      for (int j = 0; j < 12; j++) v[j] = __builtin_amdgcn_exp2f(__builtin_fmaf(v[j], cs, mc));
      const float sum = sum_rows(((v[0] + v[1]) + (v[2] + v[3])) + ((v[4] + v[5]) + (v[6] + v[7])) + ((v[8] + v[9]) + (v[10] + v[11])));
      const float inv = __builtin_amdgcn_rcpf(sum);
      pt = (u32x4){pk2(v[0] * inv, v[1] * inv), pk2(v[2] * inv, v[3] * inv), pk2(v[4] * inv, v[5] * inv), pk2(v[6] * inv, v[7] * inv)};
      pm = (u32x2){pk2(v[8] * inv, v[9] * inv), pk2(v[10] * inv, v[11] * inv)};
    }
    BAR_LDS();
    if (CLS >= 3) {
      if (CLS == 4) {
        if (lane == 0) st4c(s, r, OFF_CTRL + (C_PCH + h) * 128, 0);
        if ((s & 7) == 0) STAMP(7, c.role, 16 + s);
        if (s > 0 && lane < 40) {
          const unsigned pb = comm_base(comm, pol, s - 1);
          st16c(MARK, r, so0, pb); st16c(MARK, r, so0, pb + CM_REP);
        }
      }
      if (CLS == 3) {
        const unsigned sb = OFF_PT + (unsigned)((s * NH + h) * 2 + wave - 5) * PTT;
        st16c(pt, r, so0, sb);
        if (g >= 2) st8c(pm, r, so1, sb);
      }
      if (!ahead && s + 1 < NSEG) {
        pr.need(c, s + 1);
#pragma unroll
        for (int i = 0; i < NT; i++) t[i] = ld16c(r, to + i * 1024u, OFF_QKT + (unsigned)((s + 1) * NH + h) * 16u * KB);
      }
#pragma unroll
      for (int i = 0; i < NT; i++) *(u32x4*)(c.lds + 18432 + ((s + 1) & 1) * 16384 + to + i * 1024) = t[i];
    }
    FSAVE(s);
  }
}
DEV void role_S(const Ctx& c, int h, unsigned comm) {
  u32x4 w[40];
  load_weights<0>(c, w, c.p.Wq + h * 128, 4096, 128 * 4096, c.p.Wk + h * 128, 4096, 128 * 4096, -1);
  STAMP(5, c.role, 6);
  const unsigned pol = ring_ready(c);
  if (c.wave == 0) role_S_loop<0>(c, w, h, comm, pol);
  else if (c.wave < 4) role_S_loop<1>(c, w, h, comm, pol);
  else if (c.wave == 4) role_S_loop<2>(c, w, h, comm, pol);
  else if (c.wave < 7) role_S_loop<3>(c, w, h, comm, pol);
  else role_S_loop<4>(c, w, h, comm, pol);
  BAR_ALL();
  if (c.wave == 7 && c.lane == 0) st4c(NSEG, c.r, OFF_CTRL + (C_PCH + h) * 128, 0);
  FDUMP();
}

template <int HALF>
DEV void role_V_loop(const Ctx& c, const u32x4 (&w)[40], int h, unsigned comm, unsigned pol) {
  const int lane = c.lane, wave = c.wave, n = lane & 15, g = lane >> 4;
  const rsrc_t r = c.r;
  const int delay = TUNEV(3, 2220);
  const int idx = (lane & 2) ? 0xEEEE : 0x4444;
  const unsigned ga0 = comm_off(CM_M + wave * 2048u + lane * 16u);
  unsigned char* const gd = c.lds + wave * 2048 + lane * 16;
  const unsigned char* const asrc = c.lds + (2 * g + ((n >> 1) & 1)) * 128 + (2 * (n >> 2) + (n & 1)) * 16;
  unsigned char* const vd = c.lds + 10240 + HALF * 1024 + (4 * g + 2 * ((n >> 1) & 1) + 16 * (wave & 3)) * 16 + (2 * (n >> 2) + (n & 1)) * 2;
  const unsigned z0o = comm_off(CM_Z + (unsigned)(8 * g + wave) * 8192u + h * 256u + n * 16u), z1o = comm_off(CM_Z + (unsigned)(32 + wave) * 8192u + h * 256u + n * 16u + g * 4u);
  const unsigned k0o = (unsigned)(8 * g + wave) * 256u + n * 16u, k1o = (unsigned)(32 + wave) * 256u + n * 16u + g * 4u;
  unsigned cgo = now();
  if (HALF == 0) __builtin_amdgcn_s_setprio(2);
  for (int s = 0; s < NSEG; s++) {
    FDECL();
    unsigned base = comm_base(comm, pol, s);
    asm volatile("" : "+s"(base));
    if (HALF == 1 && wave == 7) Clock::spin_cyc(cgo);
    FSTAMP(2);
    BAR_LDS();
    if (HALF == 0 || wave == 4) gather2(c, base, ga0, gd);
    FSTAMP(0);
    BAR_LDS();
    f32x4 d0 = {0, 0, 0, 0}, d1 = {0, 0, 0, 0};
    {
      u32x4 a[10];
#pragma unroll
      for (int ks = 0; ks < 10; ks++) a[ks] = *(const u32x4*)(asrc + ks * 1024);
      SCHED();
#pragma unroll
      for (int ks = 0; ks < 10; ks += 2) {
        d0 = mfma8(a[ks], w[2 * ks], w[2 * ks + 1], d0, idx);
        d1 = mfma8(a[ks + 1], w[2 * ks + 2], w[2 * ks + 3], d1, idx);
      }
      SCHED();
    }
    const f32x2 fv = fold8(d0 + d1);
    const unsigned vv = pk2(fv[0], fv[1]);
    *(unsigned short*)vd = (unsigned short)vv;
    *(unsigned short*)(vd + 16) = (unsigned short)(vv >> 16);
    f32x4 y[5] = {};
    asm volatile("" : "+v"(y[0]), "+v"(y[1]), "+v"(y[2]), "+v"(y[3]), "+v"(y[4]));
    FSTAMP(1);
    BAR_LDS();
    const u32x4 a0 = *(const u32x4*)(c.lds + 10240 + lane * 16), a1 = *(const u32x4*)(c.lds + 11264 + lane * 16);
    SCHED();
    unsigned z[5];
#pragma unroll
    for (int t = 0; t < 5; t++) {
      y[t] = mfma8(a0, w[20 + 4 * t], w[21 + 4 * t], y[t], idx);
      y[t] = mfma8(a1, w[22 + 4 * t], w[23 + 4 * t], y[t], idx);
      if (t > 1) { const f32x2 f = fold8(y[t - 2]); z[t - 2] = nosent(pk2(f[0], f[1])); }
      SCHED();
    }
    { const f32x2 f = fold8(y[3]); z[3] = nosent(pk2(f[0], f[1])); }
    const auto q0 = __builtin_amdgcn_permlane32_swap(z[0], z[2], false, false), q1 = __builtin_amdgcn_permlane32_swap(z[1], z[3], false, false);
    const auto q2 = __builtin_amdgcn_permlane16_swap(q0[0], q1[0], false, false), q3 = __builtin_amdgcn_permlane16_swap(q0[1], q1[1], false, false);
    const u32x4 zz = {q2[0], q2[1], q3[0], q3[1]};
    { const f32x2 f = fold8(y[4]); z[4] = nosent(pk2(f[0], f[1])); }
    st16c(zz, r, z0o, base);
    st4c(z[4], r, z1o, base);
    FSTAMP(3);
    BAR_LDS();
    if (HALF == 1 && wave == 7) cgo = now() + delay;
    BAR_ALL();
    if (HALF == 1 && wave == 7 && lane == 0) st4c(s, r, OFF_CTRL + (C_PCH + 32 + h) * 128, 0);
    st16c(zz, r, k0o, OFF_ZM + (unsigned)(s * NH + h) * 10240u);
    st4c(z[4], r, k1o, OFF_ZM + (unsigned)(s * NH + h) * 10240u);
    FSAVE(s);
  }
}
DEV void role_V(const Ctx& c, int h, unsigned comm) {
  u32x4 w[40];
  load_weights<2>(c, w, c.p.Wv + h * 128, 4096, 128 * 4096, c.p.Wo + (size_t)h * 128 * DM, DM, 128, -1);
  STAMP(5, c.role, 6);
  const unsigned pol = ring_ready(c);
  if (c.wave < 4) role_V_loop<0>(c, w, h, comm, pol); else role_V_loop<1>(c, w, h, comm, pol);
  BAR_ALL();
  if (c.wave == 7 && c.lane == 0) st4c(NSEG, c.r, OFF_CTRL + (C_PCH + 32 + h) * 128, 0);
  FDUMP();
}

template <int CLS>
DEV void role_X_loop(const Ctx& c, int T, unsigned comm, unsigned pol) {
  const int lane = c.lane, wave = c.wave, n = lane & 15, g = lane >> 4;
  const rsrc_t r = c.r;
  Prog pr; pr.init(C_PVZ + 4 * wave, 4, lane);
  const int margin = TUNEV(2, 3000), sdel = TUNEV(5, 0);
  const int idx = (lane & 2) ? 0xEEEE : 0x4444;
  const unsigned po = (unsigned)(lane & 31) * 16u;
  const unsigned pa0 = comm_off(CM_P + (4 * wave + (g >> 1)) * 640u + po), pa1 = comm_off(CM_P + (4 * wave + 2 + (g >> 1)) * 640u + po);
  const unsigned pb = comm_off(CM_P + (4 * wave + g) * 640u + 512u + (2 * (n >> 2) + (n & 1)) * 16u);
  const unsigned za = comm_off(CM_Z + T * 8192u + wave * 1024u + lane * 16u);
  const unsigned mo = comm_off(CM_M + T * 256u + lane * 4u);
  float* const rw = (float*)c.lds + wave * 128 + g * 32 + n;
  const float* const rr = (const float*)c.lds + ((lane >> 2) & 7) * 16 + (lane & 3) * 4 + (lane >> 5) * 2;
  unsigned tp = now() - margin;
  for (int s = 0; s + 1 < NSEG; s++) {
    FDECL();
    pr.need(c, s);
    unsigned base = comm_base(comm, pol, s), nbase = comm_base(comm, pol, s + 1);
    asm volatile("" : "+s"(base), "+s"(nbase));
    if (CLS == 0) Clock::spin_cyc(tp + sdel);
    BAR_LDS();
    u32x4 zt[4];
#pragma unroll
    for (int i = 0; i < 4; i++) zt[i] = ld16c(r, lane * 16u, OFF_ZT + (unsigned)((s * NH + 4 * wave + i) * 40 + T) * KB);
    pr.refresh(c, s + 1);
    if ((s & 7) == 0) STAMP(7, c.role, 16 + s);
    {
      const unsigned pb_ = comm_base(comm, pol, s + 3);
      st16c(MARK, r, za, pb_);
      if (CLS == 1 && wave == 1 && lane < 48) st16c(MARK, r, comm_off(CM_M + T * 256u + n * 16u) + (g == 2 ? CM_REP3 : g * CM_REP), pb_);
    }
    if (CLS == 0) {
      GSTAMP(s, 0);
      Clock::spin_cyc(tp + margin);
    }
    FSTAMP(1);
    BAR_LDS();
    u32x4 p0 = ld16c(r, pa0, base), p1 = ld16c(r, pa1, base), pm = ld16c(r, pb, base), zm = ld16c(r, za, base);
    auto combine = [&]() {
      f32x4 acc = mfma8(p0, zt[0], zt[1], (f32x4){0, 0, 0, 0}, idx);
      acc = mfma8(p1, zt[2], zt[3], acc, idx);
      unsigned m = umax(max4(p0), max4(p1));
      SCHED();
      const f32x4 am = mfma16(pm, zm, (f32x4){0, 0, 0, 0});
      const f32x2 fa = fold8(acc);
      m = umax(m, umax(max4(pm), max4(zm)));
      SCHED();
      const f32x2 f = fa + (f32x2){am[0], am[1]};
      rw[0] = f[0];
      rw[16] = f[1];
      return m;
    };
    const unsigned m0 = combine();
    if (__builtin_expect(__builtin_amdgcn_ballot_w64(m0 == SENT) != 0, 0)) {
#pragma unroll 1
      do {
        FENCE();
        p0 = ld16c(r, pa0, base + CM_REP); p1 = ld16c(r, pa1, base + CM_REP); pm = ld16c(r, pb, base + CM_REP); zm = ld16c(r, za, base);
      } while (any_unwritten(combine(), c.budget));
    }
    DRAIN();
    FSTAMP(2);
    BAR_LDS();
    if (CLS == 0) {
      f32x2 u[8];
#pragma unroll
      for (int i = 0; i < 8; i++) u[i] = *(const f32x2*)(rr + i * 128);
      const f32x2 tt = ((u[0] + u[1]) + (u[2] + u[3])) + ((u[4] + u[5]) + (u[6] + u[7]));
      const unsigned o = nosent(pk2(tt[0], tt[1]));
      st4c(o, r, mo, nbase);
      FSTAMP(3);
      st4c(o, r, mo, nbase + CM_REP);
      st4c(o, r, mo, nbase + CM_REP3);
      tp = now();
    }
    FSAVE(s);
  }
}
DEV int xcol(int cg, int c) { return 16 * (cg >> 1) + 4 * (c >> 1) + 2 * (cg & 1) + (c & 1); }
DEV void role_X(const Ctx& c, int T, unsigned comm) {
  const int tid = c.wave * 64 + c.lane;
  if (T < 2) {
    const unsigned cal = comm + (unsigned)T * 2u * MB + 126u * 16384u;
    if (c.wave < 2) st4c(1u, c.r, tid * 128u, cal);
    BAR_ALL();
    unsigned* sl = (unsigned*)c.lds;
    if (c.wave >= 4) {
      unsigned b0 = 0xFFFFFFFFu, b1 = 0xFFFFFFFFu, acc = 0;
#pragma unroll 1
      for (int i = 0; i < 12; i++) {
        const unsigned t0 = now();
        acc += ld4c(c.r, ((i ^ c.wave) & 1) * 8192u + ((c.wave - 4) * 16 + (i >> 1) * 2) * 128u, cal);
        asm volatile("" : "+v"(acc));
        const unsigned d = now() - t0;
        if ((i ^ c.wave) & 1) b1 = d < b1 ? d : b1; else b0 = d < b0 ? d : b0;
      }
      if (c.lane == 0) { sl[c.wave] = b0; sl[8 + c.wave] = b1; }
    }
    ctrl_ready(c);
    BAR_LDS();
    if (tid == 0) {
      const unsigned b0 = min(min(sl[4], sl[5]), min(sl[6], sl[7])), b1 = min(min(sl[12], sl[13]), min(sl[14], sl[15]));
      unsigned pol = (b1 < b0 ? 1u : 0u) ^ (xcc_id() >> 2);
      st4c(pol + 1u, c.r, OFF_CTRL + (C_POL + T) * 128, 0);
    }
    BAR_LDS();
  } else {
    for (int u = (T - 2) * 512 + tid; u < 202 * 1024; u += (NX - 2) * 512) {
      const int blk = u >> 10, pg = blk >= 102;
      u32x4 val = MARK;
      const unsigned lo = ((unsigned)blk & 3u) * 8192u + ((unsigned)u & 511u) * 16u;
      if ((blk < 8 || blk == 48 || blk == 49) && lo < 10240u) {
        const int q = lo >> 4;
        const unsigned* m = (const unsigned*)(c.p.mem0 + (q & 7) * DM + xcol(q >> 3, 0));
        val = (u32x4){nosent(m[0]), nosent(m[2]), nosent(m[4]), nosent(m[6])};
      }
      st16c(val, c.r, (unsigned)pg * 2u * MB + (unsigned)(u - pg * 102 * 1024) * 16u, comm);
    }
    ctrl_ready(c);
    BAR_ALL();
    if (tid == 0) atomic_inc(c.p.ws, C_FILL);
    u32x4 v[9];
#pragma unroll
    for (int i = 0; i < 9; i++) { const int u = (i * (NX - 2) + T - 2) * 512 + tid; v[i] = *(const u32x4*)(c.p.x + (size_t)(u < 163840 ? u : 0) * 8); }
#pragma unroll
    for (int i = 0; i < 9; i++) asm volatile("" ::"v"(v[i]));
  }
  STAMP(5, c.role, 6);
  const unsigned pol = ring_ready(c);
  if (c.wave == 0) role_X_loop<0>(c, T, comm, pol);
  else role_X_loop<1>(c, T, comm, pol);
  FDUMP();
}

template <int N, int D, class A, class F>
DEV void piped(A addr, F use) {
  u32x4 b[D];
#pragma unroll
  for (int i = 0; i < D; i++) b[i] = *(const u32x4*)addr(i);
  SCHED();
#pragma unroll
  for (int i = 0; i < N; i++) {
    use(i, b[i % D]);
    if (i + D < N) b[i % D] = *(const u32x4*)addr(i + D);
    SCHED();
  }
}

template <int HALF>
DEV void role_QK_loop(const Ctx& c, const u32x4 (&w)[40], int h, int set, int cnt) {
  const int lane = c.lane, wave = c.wave;
  for (int j = 0; j < cnt; j++) {
    const int s = seg_of(set, j), nx = seg_of(set, j + 1 < cnt ? j + 1 : j);
    if (HALF == 0) stage_x(c, nx, (j + 1) & 1);
    const XTile xb(c, j & 1);
    const unsigned b = OFF_QKT + (unsigned)(s * NH + h) * 16u * KB + HALF * 8192u + (wave & 3) * 1024u;
    f32x4 d[2][2] = {};
    piped<40, 8>([&](int i) { return xb.at(i / 20, i % 20); }, [&](int i, const u32x4& xf) {
      d[i / 20][0] = mfma16(w[i % 20], xf, d[i / 20][0]);
      d[i / 20][1] = mfma16(w[20 + i % 20], xf, d[i / 20][1]);
      if (HALF == 1 && i == 12) stage_x(c, nx, (j + 1) & 1);
    });
#pragma unroll
    for (int rt = 0; rt < 2; rt++)
      st16c((u32x4){pk2(d[rt][0][0], d[rt][1][0]), pk2(d[rt][0][1], d[rt][1][1]), pk2(d[rt][0][2], d[rt][1][2]), pk2(d[rt][0][3], d[rt][1][3])}, c.r, lane * 16u, b + rt * 4096u);
    if (j < 2) BAR_ALL(); else WAIT_BAR(2);
    if (wave == 0 && lane == 0) st4c(j < 2 ? j + 1 : j, c.r, OFF_CTRL + (C_PQK + set * 32 + h) * 128, 0);
    STAMP(5, c.role, 16 + j);
  }
}
DEV void role_QK(const Ctx& c, int h, int set) {
  u32x4 w[40];
  load_weights<0>(c, w, c.p.Wq + h * 128, 4096, 128 * 4096, c.p.Wk + h * 128, 4096, 128 * 4096, set);
  STAMP(5, c.role, 6);
  BAR_ALL();
  const int cnt = set ? KH : NSEG - KH;
  if (c.wave < 4) role_QK_loop<0>(c, w, h, set, cnt); else role_QK_loop<1>(c, w, h, set, cnt);
  BAR_ALL();
  if (c.wave == 0 && c.lane == 0) st4c(cnt, c.r, OFF_CTRL + (C_PQK + set * 32 + h) * 128, 0);
}

template <int HALF>
DEV void role_VZ_loop(const Ctx& c, const u32x4 (&w)[40], int h, int set, int cnt) {
  const int lane = c.lane, wave = c.wave, n = lane & 15, g = lane >> 4;
  const unsigned vo = 40960 + ((n >> 1) & 1) * 4096 + (wave >> 1) * 1024 + (8 * (n >> 3) + 2 * ((n >> 2) & 1) + (n & 1) + 16 * (2 * (wave & 1) + (g >> 1))) * 16 + (g & 1) * 8;
  auto stage1 = [&](int s) {
    const XTile xb(c, s & 1);
    f32x4 d0 = {0, 0, 0, 0}, d1 = {0, 0, 0, 0};
    piped<40, 10>([&](int i) { return xb.at(i & 1, i >> 1); }, [&](int i, const u32x4& xf) {
      if (i & 1) d1 = mfma16(w[i >> 1], xf, d1); else d0 = mfma16(w[i >> 1], xf, d0);
    });
    unsigned char* dst = c.lds + vo + (s & 1) * 8192;
    *(u32x2*)dst = (u32x2){pk2(d0[0], d0[1]), pk2(d0[2], d0[3])};
    *(u32x2*)(dst + 64) = (u32x2){pk2(d1[0], d1[1]), pk2(d1[2], d1[3])};
  };
  auto stage2 = [&](int s) {
    u32x4 va[8];
#pragma unroll
    for (int i = 0; i < 8; i++) va[i] = *(const u32x4*)(c.lds + 40960 + (s & 1) * 8192 + i * 1024 + lane * 16);
    SCHED();
    const unsigned b = OFF_ZT + (unsigned)((seg_of(set, s) * NH + h) * 40 + wave) * KB;
    f32x4 z[5][2] = {};
#pragma unroll
    for (int t = 0; t < 5; t++) {
#pragma unroll
      for (int ke = 0; ke < 4; ke++) {
        z[t][0] = mfma16(va[ke], w[20 + t * 4 + ke], z[t][0]);
        z[t][1] = mfma16(va[4 + ke], w[20 + t * 4 + ke], z[t][1]);
      }
      if (t > 0) st16c((u32x4){pk2(z[t - 1][0][0], z[t - 1][0][1]), pk2(z[t - 1][0][2], z[t - 1][0][3]), pk2(z[t - 1][1][0], z[t - 1][1][1]), pk2(z[t - 1][1][2], z[t - 1][1][3])}, c.r, lane * 16u, b + (t - 1) * 8192u);
      SCHED();
    }
    st16c((u32x4){pk2(z[4][0][0], z[4][0][1]), pk2(z[4][0][2], z[4][0][3]), pk2(z[4][1][0], z[4][1][1]), pk2(z[4][1][2], z[4][1][3])}, c.r, lane * 16u, b + 4 * 8192u);
  };
  stage_x(c, seg_of(set, 1), 1);
  stage1(0);
  BAR_ALL();
  stage2(0);
  BAR_ALL();
  if (wave == 0 && lane == 0) st4c(1, c.r, OFF_CTRL + (C_PVZ + set * 32 + h) * 128, 0);
  STAMP(5, c.role, 16);
  stage_x(c, seg_of(set, 2), 0);
  stage1(1);
  BAR_ALL();
  for (int s = 2; s < cnt; s++) {
    const int nx = seg_of(set, s + 1 < cnt ? s + 1 : s);
    if (HALF == 0) { stage_x(c, nx, (s + 1) & 1); stage1(s); stage2(s - 1); }
    else { stage2(s - 1); stage_x(c, nx, (s + 1) & 1); stage1(s); }
    if (s < 3 || HALF == 1) BAR_ALL(); else WAIT_BAR(5);
    if (wave == 0 && lane == 0) st4c(s < 3 ? s : s - 1, c.r, OFF_CTRL + (C_PVZ + set * 32 + h) * 128, 0);
    STAMP(5, c.role, 16 + s - 1);
  }
  stage2(cnt - 1);
}
DEV void role_VZ(const Ctx& c, int h, int set) {
  u32x4 w[40];
  load_weights<2>(c, w, c.p.Wv + h * 128, 4096, 128 * 4096, c.p.Wo + (size_t)h * 128 * DM, DM, 128, set);
  STAMP(5, c.role, 6);
  BAR_ALL();
  const int cnt = set ? KH : NSEG - KH;
  if (c.wave < 4) role_VZ_loop<0>(c, w, h, set, cnt); else role_VZ_loop<1>(c, w, h, set, cnt);
  BAR_ALL();
  if (c.wave == 0 && c.lane == 0) st4c(cnt, c.r, OFF_CTRL + (C_PVZ + set * 32 + h) * 128, 0);
}

constexpr float CSA = 0.22360679774997896f * 1.4426950408889634f;
DEV void role_qkv(const Ctx& c, int a) {
  const int lane = c.lane, wave = c.wave, n = lane & 15, g = lane >> 4;
  const rsrc_t r = c.r;
  const int cgp = a & 7, s0 = (a >> 3) * QSEG, s1 = s0 + QSEG < NSEG ? s0 + QSEG : NSEG;
  const int sl = cgp * 8 + wave;
  for (int i = TUNEV(4, 0); i > 0; i--) __builtin_amdgcn_s_sleep(32);
  u32x4 w[40];
  load_weights<0>(c, w, c.p.wqkv + cgp * 256, 1920, 128 * 1920, c.p.wqkv + cgp * 256 + (cgp < 7 ? 128 : 0), 1920, 128 * 1920, s0, cgp >= 5);
  if (sl == 60) {
#pragma unroll
    for (int ks = 0; ks < 20; ks++) w[ks] = *(const u32x4*)(c.p.gw + ks * 32 + g * 8);
  }
  BAR_ALL();
  const int c8 = (sl < 20 ? sl : sl - 20) * 4 + g, grp5 = c8 % 5, hq = (c8 / 5) * 2 + (grp5 > 2);
  const unsigned tile = sl < 20 ? QAT : KVT, qkb = (sl < 20 ? OFF_QA : OFF_KV) + (unsigned)hq * 64u * tile;
  for (int s = s0; s < s1; s++) {
    if (s + 1 < s1) stage_x(c, s + 1, (s + 1) & 1);
    const XTile xb(c, s & 1);
    if (sl < 40 || sl == 60) {
      f32x4 d[2][2] = {};
      piped<40, 8>([&](int i) { return xb.at(i / 20, i % 20); }, [&](int i, const u32x4& xf) {
        d[i / 20][0] = mfma16(w[i % 20], xf, d[i / 20][0]);
        d[i / 20][1] = mfma16(w[20 + i % 20], xf, d[i / 20][1]);
      });
      DRAIN();
      if (sl == 60) {
        const float z = rbf(g & 1 ? d[1][0][0] : d[0][0][0]);
        if (g < 2) st4c(__float_as_uint(rbf(1.f / (1.f + __expf(-z)))), r, OFF_GATE + (unsigned)(s * 32 + lane) * 4u, 0);
      } else {
        const float sc = sl < 20 ? CSA : 1.f;
#pragma unroll
        for (int rt = 0; rt < 2; rt++) {
          const int row = sl < 20 ? rt * 16 + n : 16 * (n >> 3) + 8 * rt + (n & 7);
          const u32x4 v = {pk2(d[rt][0][0] * sc, d[rt][0][1] * sc), pk2(d[rt][0][2] * sc, d[rt][0][3] * sc), pk2(d[rt][1][0] * sc, d[rt][1][1] * sc), pk2(d[rt][1][2] * sc, d[rt][1][3] * sc)};
          if (grp5 != 2) st16c(v, r, qkb + (unsigned)(row + 32 * (grp5 == 1 || grp5 == 4)) * 16u, (unsigned)s * tile);
          else {
            st8c((u32x2){v.x, v.y}, r, qkb + 1024u + row * 8u, (unsigned)s * tile);
            st8c((u32x2){v.z, v.w}, r, qkb + 64u * tile + 1024u + row * 8u, (unsigned)s * tile);
          }
        }
      }
    } else if (sl < 60) {
      f32x4 d[2][2] = {};
      piped<40, 8>([&](int i) { return xb.at(i / 20, i % 20); }, [&](int i, const u32x4& xf) {
        d[i / 20][0] = mfma16(xf, w[i % 20], d[i / 20][0]);
        d[i / 20][1] = mfma16(xf, w[20 + i % 20], d[i / 20][1]);
      });
      DRAIN();
#pragma unroll
      for (int t = 0; t < 2; t++) {
        const int cc = (sl - 40) * 32 + t * 16 + n, hh = cc / 20, dd = cc - hh * 20;
        st16c((u32x4){pk2(d[0][t][0], d[0][t][1]), pk2(d[0][t][2], d[0][t][3]), pk2(d[1][t][0], d[1][t][1]), pk2(d[1][t][2], d[1][t][3])}, r,
              OFF_KV + (unsigned)(hh * 64) * KVT + 1280u + (unsigned)(g * 384 + dd * 16), (unsigned)s * KVT);
      }
    } else {
      DRAIN();
      const int u = (sl & 1) * 64 + lane;
      st16c((u32x4){0x3F803F80u, 0x3F803F80u, 0x3F803F80u, 0x3F803F80u}, r, OFF_KV + (unsigned)((u >> 2) * 64 + s) * KVT + 1280u + (unsigned)(u & 3) * 384u + 320u, 0);
    }
    BAR_LDS();
  }
  for (int f = a * 8 + wave; f < 800; f += NA * 8) {
    const unsigned short* q = (const unsigned short*)(c.p.wo + (size_t)((f % 20) * 32 + g * 8) * DM + (f / 20) * 16 + n);
    unsigned v[8];
#pragma unroll
    for (int j = 0; j < 8; j++) v[j] = q[j * DM];
    st16c((u32x4){v[0] | (v[1] << 16), v[2] | (v[3] << 16), v[4] | (v[5] << 16), v[6] | (v[7] << 16)}, r, lane * 16u, OFF_WO + (unsigned)f * KB);
  }
  BAR_ALL();
  if (wave == 0 && lane == 0) atomic_inc(c.p.ws, C_QKVDONE);
}

DEV int take_ticket(const Ctx& c, int line) {
  unsigned* slot = (unsigned*)(c.lds + 160 * 1024 - 16);
  BAR_LDS();
  if (c.wave == 0 && c.lane == 0) *slot = atomic_inc(c.p.ws, line);
  BAR_ALL();
  return __builtin_amdgcn_readfirstlane(*slot);
}

DEV f32x16 qk32(u32x4 k0, u32x2 k1, u32x4 q0, u32x2 q1) {
  f32x16 z;
#pragma unroll
  for (int i = 0; i < 16; i++) z[i] = 0;
  z = __builtin_amdgcn_mfma_f32_32x32x16_bf16(as8(k0), as8(q0), z, 0, 0, 0);
  asm volatile("s_nop 11" : "+v"(z));
  return __builtin_amdgcn_mfma_f32_32x32x8bf16_1k(__builtin_bit_cast(s16x4, k1), __builtin_bit_cast(s16x4, q1), z, 0, 0, 0);
}
DEV u32x2 split_neg(float m) {
  const float h1 = rbf(m), h2 = rbf(m - h1), h3 = rbf((m - h1) - h2);
  return (u32x2){pk2(-h1, -h2), pk2(-h3, 0.f)};
}
DEV float max16(const f32x16& s) {
  float mx = s[0];
#pragma unroll
  for (int i = 1; i < 16; i++) mx = __builtin_fmaxf(mx, s[i]);
  return max_halves(mx);
}
DEV u32x4 exp8(const f32x16& s, int o, float sh) {
  float e[8];
#pragma unroll
  for (int j = 0; j < 8; j++) e[j] = __builtin_amdgcn_exp2f(s[o + j] - sh);
  return (u32x4){pk2(e[0], e[1]), pk2(e[2], e[3]), pk2(e[4], e[5]), pk2(e[6], e[7])};
}
DEV f32x16 pv32(u32x4 v, u32x4 p, const f32x16& o) { return __builtin_amdgcn_mfma_f32_32x32x16_bf16(as8(v), as8(p), o, 0, 0, 0); }

DEV void attn_unit(const Ctx& c, int h, int qb) {
  const int lane = c.lane, wave = c.wave, tid = wave * 64 + lane, q = lane & 31, hf = lane >> 5;
  const rsrc_t r = c.r;
  constexpr int CH = 8 * KVT;
  const unsigned qo = OFF_QA + (unsigned)(h * 64 + qb * 8 + wave) * QAT;
  const u32x4 q0 = ld16c(r, lane * 16u, qo);
  u32x2 q1 = ld8c(r, 1024u + q * 8u, qo);
  if (hf) q1 = (u32x2){0, 0};
  const unsigned kvo = OFF_KV + (unsigned)(h * 64) * KVT;
  const bool third = tid < (CH / 16 - 1024);
  u32x4 st0 = ld16c(r, tid * 16u, kvo), st1 = ld16c(r, tid * 16u + 8192u, kvo), st2 = ld16c(r, (third ? tid : 0) * 16u + 16384u, kvo);
  *(u32x4*)(c.lds + tid * 16) = st0; *(u32x4*)(c.lds + tid * 16 + 8192) = st1;
  if (third) *(u32x4*)(c.lds + tid * 16 + 16384) = st2;
  BAR_LDS();
  const int k1o = hf ? 1280 + 320 : 1024 + q * 8, vo = 1280 + hf * 384 + (q < 20 ? q : 20) * 16;
  float m = max16(qk32(*(const u32x4*)(c.lds + lane * 16), *(const u32x2*)(c.lds + k1o), q0, q1));
  if (hf) q1 = split_neg(m);
  f32x16 o;
#pragma unroll
  for (int i = 0; i < 16; i++) o[i] = 0;
  for (int ch = 0; ch < 8; ch++) {
    if (ch + 1 < 8) {
      const unsigned b = kvo + (unsigned)(ch + 1) * CH;
      st0 = ld16c(r, tid * 16u, b); st1 = ld16c(r, tid * 16u + 8192u, b); st2 = ld16c(r, (third ? tid : 0) * 16u + 16384u, b);
    }
    const unsigned char* kb = c.lds + (ch & 1) * CH;
    const f32x16 keep = o;
    {
      u32x4 k0 = *(const u32x4*)(kb + lane * 16); u32x2 k1 = *(const u32x2*)(kb + k1o);
      f32x16 sc = qk32(k0, k1, q0, q1);
      k0 = *(const u32x4*)(kb + KVT + lane * 16); k1 = *(const u32x2*)(kb + KVT + k1o);
#pragma unroll
      for (int t = 0; t < 8; t++) {
        const u32x4 v0 = *(const u32x4*)(kb + t * KVT + vo), v1 = *(const u32x4*)(kb + t * KVT + vo + 768);
        f32x16 sn;
#pragma unroll
        for (int i = 0; i < 16; i++) sn[i] = 0;
        float e[8];
        SCHED();
#pragma unroll
        for (int j = 0; j < 4; j++) e[j] = __builtin_amdgcn_exp2f(sc[j]);
        SCHED();
        if (t + 1 < 8) sn = __builtin_amdgcn_mfma_f32_32x32x16_bf16(as8(k0), as8(q0), sn, 0, 0, 0);
        SCHED();
#pragma unroll
        for (int j = 4; j < 8; j++) e[j] = __builtin_amdgcn_exp2f(sc[j]);
        const u32x4 pa = {pk2(e[0], e[1]), pk2(e[2], e[3]), pk2(e[4], e[5]), pk2(e[6], e[7])};
        SCHED();
        if (t + 1 < 8) sn = __builtin_amdgcn_mfma_f32_32x32x8bf16_1k(__builtin_bit_cast(s16x4, k1), __builtin_bit_cast(s16x4, q1), sn, 0, 0, 0);
        if (t + 2 < 8) { k0 = *(const u32x4*)(kb + (t + 2) * KVT + lane * 16); k1 = *(const u32x2*)(kb + (t + 2) * KVT + k1o); }
        SCHED();
#pragma unroll
        for (int j = 0; j < 4; j++) e[j] = __builtin_amdgcn_exp2f(sc[8 + j]);
        SCHED();
        o = pv32(v0, pa, o);
        SCHED();
#pragma unroll
        for (int j = 4; j < 8; j++) e[j] = __builtin_amdgcn_exp2f(sc[8 + j]);
        const u32x4 pb = {pk2(e[0], e[1]), pk2(e[2], e[3]), pk2(e[4], e[5]), pk2(e[6], e[7])};
        SCHED();
        o = pv32(v1, pb, o);
        if (t + 1 < 8) sc = sn;
      }
    }
    if (__builtin_amdgcn_ballot_w64(!(o[12] < 1.0e9f)) != 0) {
      o = keep;
#pragma unroll 1
      for (int t = 0; t < 8; t++) {
        const unsigned char* kt = kb + t * KVT;
        const f32x16 sc = qk32(*(const u32x4*)(kt + lane * 16), *(const u32x2*)(kt + k1o), q0, q1);
        const float mn = m + __builtin_fmaxf(max16(sc), 0.f), sh = mn - m, al = __builtin_amdgcn_exp2f(-sh);
#pragma unroll
        for (int i = 0; i < 16; i++) o[i] *= al;
        m = mn;
        if (hf) q1 = split_neg(m);
        o = pv32(*(const u32x4*)(kt + vo), exp8(sc, 0, sh), o);
        o = pv32(*(const u32x4*)(kt + vo + 768), exp8(sc, 8, sh), o);
      }
    }
    if (ch + 1 < 8) {
      unsigned char* nb = c.lds + ((ch + 1) & 1) * CH;
      *(u32x4*)(nb + tid * 16) = st0; *(u32x4*)(nb + tid * 16 + 8192) = st1;
      if (third) *(u32x4*)(nb + tid * 16 + 16384) = st2;
    }
    BAR_LDS();
  }
  const float inv = 1.0f / o[12];
  const unsigned ob = OFF_OF + (unsigned)(qb * 8 + wave) * 40960u, ol = (q >> 4) * 20480u + (q & 15) * 16u;
#pragma unroll
  for (int b = 0; b < 3; b++) {
    const int col = h * 20 + 8 * b + 4 * hf;
    if (b < 2 || !hf)
      st8c((u32x2){pk2(o[4 * b] * inv, o[4 * b + 1] * inv), pk2(o[4 * b + 2] * inv, o[4 * b + 3] * inv)}, r,
           ol + (unsigned)((col >> 5) * 1024 + ((col >> 3) & 3) * 256 + (col & 7) * 2), ob);
  }
  BAR_ALL();
  if (wave == 0 && lane == 0) atomic_inc(c.p.ws, C_AT + qb);
}

template <int MODE>
DEV void final_unit(const Ctx& c, int seg, int cb) {
  const int lane = c.lane, wave = c.wave, tid = wave * 64 + lane, n = lane & 15, g = lane >> 4;
  const rsrc_t r = c.r;
  const int T = cb * 8 + wave;
  if (wave == 0) {
    if (MODE != 1) wait_lines(c, C_AT + (seg >> 3), 1, 32, 1);
    if (MODE != 2) wait_lines(c, C_PVZ + seg_set(seg) * 32, 32, seg_idx(seg) + 1, 1);
    else wait_lines(c, C_BD + seg, 1, 5, 1);
  }
  BAR_LDS();
  f32x4 a0 = {0, 0, 0, 0}, a1 = {0, 0, 0, 0}, b0 = {0, 0, 0, 0}, b1 = {0, 0, 0, 0};
  float g0 = 0, g1 = 0;
  u32x4 zt[32], bp;
  constexpr int ZE = MODE == 0 ? 20 : 0;
  if (MODE != 1) {
    u32x4 t[5], wf[20];
#pragma unroll
    for (int i = 0; i < 5; i++) t[i] = ld16c(r, tid * 16u, OFF_OF + (unsigned)seg * 40960u + i * 8192u);
#pragma unroll
    for (int i = 0; i < 20; i++) wf[i] = ld16c(r, lane * 16u + (i & 3) * 1024u, OFF_WO + (unsigned)T * 20480u + (i >> 2) * 4096u);
#pragma unroll
    for (int h = 0; h < ZE; h++) zt[h] = ld16c(r, lane * 16u, OFF_ZT + (unsigned)((seg * NH + h) * 40 + T) * KB);
    if (MODE == 2) bp = ld16c(r, lane * 16u, OFF_BP + (unsigned)(seg * 40 + T) * KB);
    g0 = __uint_as_float(ld4c(r, n * 4u, OFF_GATE + seg * 128u)); g1 = __uint_as_float(ld4c(r, n * 4u + 64u, OFF_GATE + seg * 128u));
    SCHED();
#pragma unroll
    for (int i = 0; i < 5; i++) *(u32x4*)(c.lds + i * 8192 + tid * 16) = t[i];
    BAR_LDS();
    piped<40, 4>([&](int i) { return c.lds + ((i & 1) * 20 + (i >> 1)) * 1024 + lane * 16; }, [&](int i, const u32x4& of) {
      if (i & 1) a1 = mfma16(wf[i >> 1], of, a1); else a0 = mfma16(wf[i >> 1], of, a0);
    });
    asm volatile("" : "+v"(a0), "+v"(a1));
  }
  if (MODE != 2) {
#pragma unroll
    for (int h = ZE; h < 32; h++) zt[h] = ld16c(r, lane * 16u, OFF_ZT + (unsigned)((seg * NH + h) * 40 + T) * KB);
    if (wave == 0) { wait_lines(c, C_PCH, 1, seg, 1); wait_lines(c, C_PCH, 64, seg + 1, 0); }
    BAR_LDS();
    unsigned char* pl = c.lds + 40960;
    u32x4 zm[8];
    {
      u32x4 t[10];
#pragma unroll
      for (int i = 0; i < 10; i++) t[i] = ld16c(r, tid * 16u, OFF_PT + (unsigned)seg * 81920u + i * 8192u);
#pragma unroll
      for (int i = 0; i < 8; i++) zm[i] = ld16c(r, g * 10240u + n * 16u, OFF_ZM + (unsigned)(seg * NH + 4 * i) * 10240u + T * 256u);
      SCHED();
#pragma unroll
      for (int i = 0; i < 10; i++) *(u32x4*)(pl + i * 8192 + tid * 16) = t[i];
    }
    BAR_LDS();
    piped<64, 8>([&](int i) { return pl + i * PTT + lane * 16; }, [&](int i, const u32x4& pf) {
      if (i & 1) b1 = mfma16(zt[i >> 1], pf, b1); else b0 = mfma16(zt[i >> 1], pf, b0);
    });
    piped<16, 8>([&](int i) { return pl + ((4 * (i >> 1) + g) * 2 + (i & 1)) * PTT + 1024 + n * 16; }, [&](int i, const u32x4& pf) {
      if (i & 1) b1 = mfma16(zm[i >> 1], pf, b1); else b0 = mfma16(zm[i >> 1], pf, b0);
    });
    bp = (u32x4){pk2(b0[0], b0[1]), pk2(b0[2], b0[3]), pk2(b1[0], b1[1]), pk2(b1[2], b1[3])};
  }
  if (MODE == 1) {
    st16c(bp, r, lane * 16u, OFF_BP + (unsigned)(seg * 40 + T) * KB);
    BAR_ALL();
    if (tid == 0) atomic_inc(c.p.ws, C_BD + seg);
    return;
  }
  const bf16x8 bb = as8(bp);
  const float h0 = rbf(1.f - g0), h1 = rbf(1.f - g1);
  float y0[4], y1[4];
#pragma unroll
  for (int j = 0; j < 4; j++) {
    y0[j] = rbf(g0 * rbf(a0[j])) + rbf(h0 * (float)bb[j]);
    y1[j] = rbf(g1 * rbf(a1[j])) + rbf(h1 * (float)bb[4 + j]);
  }
  bf16* o = c.p.out + (size_t)(seg * 32 + n) * DM + T * 16 + 4 * g;
  *(u32x2*)o = (u32x2){pk2(y0[0], y0[1]), pk2(y0[2], y0[3])};
  *(u32x2*)(o + 16 * DM) = (u32x2){pk2(y1[0], y1[1]), pk2(y1[2], y1[3])};
}

DEV void tail_unit(const Ctx& c, int seg, int rt, int T0) {
  const int lane = c.lane, wave = c.wave, n = lane & 15, g = lane >> 4;
  const rsrc_t r = c.r;
  if (wave == 0) { wait_lines(c, C_AT + (seg >> 3), 1, 32, 1); wait_lines(c, C_PVZ + seg_set(seg) * 32, 32, seg_idx(seg) + 1, 1); }
  BAR_LDS();
  u32x4 of[3], wf[3][2], zt[4][2];
#pragma unroll
  for (int i = 0; i < 3; i++) {
    const int ks = wave + 8 * i < 20 ? wave + 8 * i : wave;
    of[i] = ld16c(r, lane * 16u, OFF_OF + (unsigned)((seg * 2 + rt) * 20 + ks) * KB);
    wf[i][0] = ld16c(r, lane * 16u, OFF_WO + (unsigned)(T0 * 20 + ks) * KB);
    wf[i][1] = ld16c(r, lane * 16u, OFF_WO + (unsigned)(T0 * 20 + 20 + ks) * KB);
  }
#pragma unroll
  for (int i = 0; i < 8; i++) zt[i >> 1][i & 1] = ld16c(r, lane * 16u, OFF_ZT + (unsigned)((seg * NH + 4 * wave + (i >> 1)) * 40 + T0 + (i & 1)) * KB);
  const float gt = __uint_as_float(ld4c(r, n * 4u, OFF_GATE + (unsigned)(seg * 32 + rt * 16) * 4u));
  if (wave >= 4) of[2] = (u32x4){0, 0, 0, 0};
  f32x4* part = (f32x4*)c.lds;
  auto total = [&](int o) {
    f32x4 u[8];
#pragma unroll
    for (int i = 0; i < 8; i++) u[i] = part[o + (i * 2 + (wave & 1)) * 64 + lane];
    return ((u[0] + u[1]) + (u[2] + u[3])) + ((u[4] + u[5]) + (u[6] + u[7]));
  };
#pragma unroll
  for (int t = 0; t < 2; t++) part[(wave * 2 + t) * 64 + lane] = mfma16(wf[2][t], of[2], mfma16(wf[1][t], of[1], mfma16(wf[0][t], of[0], (f32x4){0, 0, 0, 0})));
  BAR_LDS();
  f32x4 a = total(0);
#pragma unroll
  for (int j = 0; j < 4; j++) a[j] = rbf(gt * rbf(a[j]));
  asm volatile("" : "+v"(a), "+v"(zt[0][0]), "+v"(zt[3][1]));
  if (wave == 7) { wait_lines(c, C_PCH, 1, seg, 1); wait_lines(c, C_PCH, 64, seg + 1, 0); }
  BAR_LDS();
  u32x4 pf[4], zm[2];
#pragma unroll
  for (int i = 0; i < 4; i++) pf[i] = ld16c(r, lane * 16u, OFF_PT + (unsigned)((seg * NH + 4 * wave + i) * 2 + rt) * PTT);
  const u32x4 pm = ld16c(r, g * 2u * PTT + 1024u + n * 16u, OFF_PT + (unsigned)((seg * NH + 4 * wave) * 2 + rt) * PTT);
#pragma unroll
  for (int t = 0; t < 2; t++) zm[t] = ld16c(r, g * 10240u + n * 16u, OFF_ZM + (unsigned)(seg * NH + 4 * wave) * 10240u + (unsigned)(T0 + t) * 256u);
#pragma unroll
  for (int t = 0; t < 2; t++) {
    f32x4 b = {0, 0, 0, 0};
#pragma unroll
    for (int i = 0; i < 4; i++) b = mfma16(zt[i][t], pf[i], b);
    part[1024 + (wave * 2 + t) * 64 + lane] = mfma16(zm[t], pm, b);
  }
  BAR_LDS();
  if (wave < 2) {
    const f32x4 b = total(1024);
    const float hg = rbf(1.f - gt);
    *(u32x2*)(c.p.out + (size_t)(seg * 32 + rt * 16 + n) * DM + (T0 + wave) * 16 + 4 * g) =
        (u32x2){pk2(a[0] + rbf(hg * rbf(b[0])), a[1] + rbf(hg * rbf(b[1]))), pk2(a[2] + rbf(hg * rbf(b[2])), a[3] + rbf(hg * rbf(b[3])))};
  }
}
constexpr int S1 = NSEG - 2;
#define PLACE 0
__host__ __device__ inline void place(int grp, int slot, int& kind, int& id) {
  const int ns[4] = {16, 16, 0, 0}, nv[4] = {16, 16, 0, 0}, nx[4] = {0, 0, 20, 20};
  int s0 = 0, v0 = 0, x0 = 0, a0 = 0;
  for (int k = 0; k < 4; k++) if (k < grp) { s0 += ns[k]; v0 += nv[k]; x0 += nx[k]; a0 += 32 - ns[k] - nv[k] - nx[k]; }
  int cs = 0, cv = 0, cx = 0;
  for (int k = 0; k < 4; k++) if (k == grp) { cs = ns[k]; cv = nv[k]; cx = nx[k]; }
  if (slot < cs) { kind = 0; id = s0 + slot; }
  else if (slot < cs + cv) { kind = 1; id = v0 + slot - cs; }
  else if (slot < cs + cv + cx) { kind = 2; id = x0 + slot - cs - cv; }
  else { kind = 3; id = a0 + slot - cs - cv - cx; }
}
__global__ void __launch_bounds__(512, 2) main_kernel(Params p) {
  extern __shared__ __attribute__((aligned(16))) unsigned char lds[];
  const int lane = threadIdx.x & 63, wave = __builtin_amdgcn_readfirstlane(threadIdx.x >> 6);
  Ctx c{p, make_rsrc(p.ws, WS_USED), lds, lane, wave, 0, SPIN_CAP, 0};
  const unsigned comm = comm_start(p.ws);
  unsigned* sl = (unsigned*)(lds + 160 * 1024 - 128);
  const unsigned seed = __builtin_amdgcn_readfirstlane(ld4c(c.r, OFF_CTRL + C_SEED * 128, 0));
  c.tag = seed + 0x9E3779B9u;
  if (blockIdx.x == 0 && threadIdx.x >= 64 && threadIdx.x - 62 < C_LINES) st4c(0u, c.r, OFF_CTRL + (threadIdx.x - 62) * 128u, 0);
  if (wave == 0) {
    auto claim = [&](unsigned role) {
      unsigned old = 0;
      if (lane == 0) old = __hip_atomic_exchange((unsigned*)(p.ws + OFF_CLAIM + role * 128), c.tag, __ATOMIC_RELAXED, __HIP_MEMORY_SCOPE_AGENT);
      return __builtin_amdgcn_readfirstlane(old) != c.tag;
    };
    unsigned role = xcc_id() * 32 + (blockIdx.x >> 3);
    const bool mine = claim(role);
    if (lane == 0) st4c(c.tag, c.r, OFF_CLAIM + 32768u + blockIdx.x * 4u, 0);
    if (!mine) {
#pragma unroll 1
      for (;;) {
        FENCE();
        const u32x4 v = ld16c(c.r, OFF_CLAIM + 32768u + lane * 16u, 0);
        if (__builtin_amdgcn_ballot_w64(v.x != c.tag || v.y != c.tag || v.z != c.tag || v.w != c.tag) == 0 || --c.budget < 0) break;
      }
#pragma unroll 1
      for (role = 0; role < 256u && !claim(role); role++) {}
    }
    if (lane == 0) sl[0] = role;
  }
  if (blockIdx.x == 0) BAR_ALL(); else BAR_LDS();
  if (blockIdx.x == 0 && threadIdx.x == 64) st4c(c.tag, c.r, OFF_CTRL + C_READY * 128, 0);
  const int role = __builtin_amdgcn_readfirstlane(sl[0]);
  const int grp = role >> 5, slot = role & 31;
  if (grp > 7) return;
  c.role = role;
  STAMP(5, c.role, 1);
  int kind = 3, id = 0;
  if (grp < 4) place(grp, slot, kind, id);
  if (kind == 0) { role_S(c, id, comm); return; }
  if (kind == 1) { role_V(c, id, comm); return; }
  if (kind == 2) { role_X(c, id, comm); return; }
  if (grp < 4) role_qkv(c, id);
  else if (slot & 8) role_VZ(c, (grp - 4) * 8 + (slot & 7), slot >> 4);
  else role_QK(c, (grp - 4) * 8 + (slot & 7), slot >> 4);
  STAMP(5, c.role, 2);
  const bool follow = grp >= 4 && slot >= 16 && slot < 16 + NBX;
#pragma unroll 1
  for (int ph = follow ? 0 : 1; ph < 3; ph++) {
    if (ph == 1) {
      if (wave == 0) wait_lines(c, C_QKVDONE, 1, NA, 1);
      STAMP(0, c.role, 3);
      for (;;) {
        const int u = take_ticket(c, C_QATT);
        if (u >= 256) break;
        attn_unit(c, u & 31, ((u >> 5) + S0 / 8) & 7);
        STAMP(5, 256, u);
      }
      STAMP(5, c.role, 4);
    } else {
      for (;;) {
        const int u = take_ticket(c, C_QB);
        if (u >= S0 * 5) break;
        final_unit<1>(c, u / 5, u % 5);
        STAMP(5, 258, u);
      }
    }
  }
  for (;;) {
    const int u = take_ticket(c, C_QA);
    if (u >= S0 * 5) break;
    final_unit<2>(c, u / 5, u % 5);
    STAMP(5, 257, u);
  }
  for (;;) {
    const int u = take_ticket(c, C_QFIN) + S0 * 5;
    if (u >= S1 * 5) break;
    final_unit<0>(c, u / 5, u % 5);
    STAMP(5, 257, u);
  }
  for (;;) {
    const int u = take_ticket(c, C_QTAIL);
    if (u >= (NSEG - S1) * 40) break;
    tail_unit(c, S1 + u / 40, u & 1, ((u % 40) >> 1) * 2);
    if (u == (NSEG - S1) * 40 - 1 && threadIdx.x == 0) st4c(seed + 1u, c.r, OFF_CTRL + C_SEED * 128, 0);
    STAMP(5, 257, 320 + u);
  }
  STAMP(5, c.role, 5);
}

extern "C" void kernel_launch(void* const* d_in, const int* in_sizes, int n_in, void* d_out, int out_size, void* d_ws, size_t ws_size,
                              hipStream_t stream) {
  Params p;
  p.x = (const bf16*)d_in[0]; p.wqkv = (const bf16*)d_in[1]; p.wo = (const bf16*)d_in[2]; p.mem0 = (const bf16*)d_in[4];
  p.Wq = (const bf16*)d_in[5]; p.Wk = (const bf16*)d_in[6]; p.Wv = (const bf16*)d_in[7]; p.Wo = (const bf16*)d_in[8];
  p.gw = (const bf16*)d_in[10]; p.out = (bf16*)d_out; p.ws = (unsigned char*)d_ws;
  (void)hipFuncSetAttribute((const void*)main_kernel, hipFuncAttributeMaxDynamicSharedMemorySize, 160 * 1024);
  main_kernel<<<256, 512, 160 * 1024, stream>>>(p);
}
```

```cpp
#include <hip/hip_runtime.h>
#include <hip/hip_bf16.h>

typedef __bf16 bf16;
typedef __attribute__((ext_vector_type(2))) __bf16 bf16x2;
typedef __attribute__((ext_vector_type(8))) __bf16 bf16x8;
typedef __attribute__((ext_vector_type(16))) __bf16 bf16x16;
typedef __attribute__((ext_vector_type(4))) short s16x4;
typedef __attribute__((ext_vector_type(2))) float f32x2;
typedef __attribute__((ext_vector_type(4))) float f32x4;
typedef __attribute__((ext_vector_type(16))) float f32x16;
typedef __attribute__((ext_vector_type(2))) unsigned u32x2;
typedef __attribute__((ext_vector_type(4))) unsigned u32x4;
typedef __attribute__((ext_vector_type(8))) unsigned u32x8;
typedef __amdgpu_buffer_rsrc_t rsrc_t;
#define DEV __device__ __forceinline__
#define SC1 16

#define SSLEEP 2
#define CALW 2
#define CALN 8
constexpr int NSEG = 64, NH = 32, DM = 640;
constexpr unsigned KB = 1024u, MB = 1024u * 1024u;
constexpr unsigned OFF_CTRL = 0;
constexpr unsigned OFF_CLAIM = 128 * KB;
constexpr unsigned OFF_GATE = 64 * KB;
constexpr unsigned OFF_WO = 1 * MB;
constexpr unsigned OFF_OF = 2 * MB;
constexpr unsigned OFF_QA = 5 * MB;
constexpr unsigned OFF_KA = 8 * MB;
constexpr unsigned OFF_VA = 11 * MB;
constexpr unsigned OFF_PT = 14 * MB;
constexpr unsigned OFF_BP = 20 * MB;
constexpr unsigned OFF_ZM = 24 * MB;
constexpr unsigned OFF_COMM = 44 * MB;
constexpr unsigned WS_USED = 242 * MB;
constexpr int K_QKT = 64 * 32 * 40;
constexpr int C_SEED = 0, C_READY = 1;
constexpr int C_QTAIL = 8, C_QKVDONE = 9, C_QATT = 10, C_QFIN = 11;
constexpr int C_FILL = 12, C_WO = 13;
constexpr int C_POL = 288;
constexpr int C_AT = 16;
constexpr int C_PQK = 32;
constexpr int C_PVZ = 96;
constexpr int C_PCH = 160;
constexpr int C_QB = 24, C_QA = 25;
constexpr int C_BD = 224;
constexpr int C_LINES = 296;
#define SEG0 56
#define NBX 5
constexpr int S0 = SEG0;
constexpr int NA = 24, NX = 40, QSEG = 22;
#define KHELP 20
#define KHQ 17
#define KHF 4
constexpr int KH = KHELP;
DEV int kh_qk(int h) { return (h & 7) < NBX ? KHF : KHQ; }
DEV int seg_set(int s, int kh = KH) { return s < 2 * kh ? s & 1 : 0; }
DEV int seg_idx(int s, int kh = KH) { return s < 2 * kh ? s >> 1 : s - kh; }
DEV int seg_of(int set, int j, int kh = KH) { return set ? 2 * j + 1 : j < kh ? 2 * j : j + kh; }
constexpr unsigned SENT = 0xFFFFFFFFu;
constexpr int SPIN_CAP = 1 << 21;
constexpr int KVT = 2816, QAT = 1280, PTT = 1280;
constexpr unsigned CM_M = 0, CM_P = 10240, CM_Z = 65536, CM_REP = 4 * 16384, CM_REP3 = 48 * 16384  ;
DEV unsigned comm_off(unsigned o) { return ((o >> 13) << 14) | (o & 8191u); }
DEV unsigned comm_base(unsigned comm, unsigned pol, int s) {
  const unsigned pg = ((unsigned)s >> 1) & 1u;
  return comm + pg * 2u * MB + ((unsigned)s & 1u) * 50u * 16384u + (((pol >> pg) & 1u) << 13);
}
DEV unsigned pace_addr(unsigned comm, unsigned pol, int s) { return comm + 100u * 16384u + ((pol & 1u) << 13) + (unsigned)s * 128u; }
#define TUNEV(i, d) (d)

struct Params {
  const bf16 *x, *wqkv, *wo, *mem0, *Wq, *Wk, *Wv, *Wo, *gw;
  bf16* out;
  unsigned char* ws;
};
struct Ctx {
  const Params& p;
  rsrc_t r;
  unsigned char* lds;
  mutable int lane;
  int wave, role;
  mutable int budget;
  unsigned tag;
  mutable unsigned zbase, zpol;
  DEV void relane() const { asm volatile("v_mbcnt_lo_u32_b32 %0, -1, 0\n\tv_mbcnt_hi_u32_b32 %0, -1, %0" : "=v"(lane)); }
  DEV unsigned zt(int k) const {
    const unsigned o = (unsigned)k * 1024u, q = ((o >> 13) << 14) | (o & 8191u);
    return zbase + (q | ((((q >> 21) ^ zpol) & 1u) << 13));
  }
  DEV unsigned qkt(int s, int h, int half) const { return zt(K_QKT + ((s & 31) * NH + h) * 16 + half * 8); }
};

DEV rsrc_t make_rsrc(const void* p, unsigned bytes) { return __builtin_amdgcn_make_buffer_rsrc((void*)p, 0, bytes, 0x00020000); }
DEV unsigned comm_start(const unsigned char* ws) {
  const unsigned lo = (unsigned)((unsigned long long)ws & (2u * MB - 1));
  return OFF_COMM + ((2u * MB - lo) & (2u * MB - 1));
}
DEV unsigned xcc_id() { return (unsigned)__builtin_amdgcn_s_getreg(20 | (3 << 11)) & 7u; }
DEV unsigned pk2(float a, float b) { bf16x2 v = {(bf16)a, (bf16)b}; return __builtin_bit_cast(unsigned, v); }
DEV unsigned nosent(unsigned v) { return v < 0xFFFFFFFEu ? v : 0xFFFFFFFEu; }
DEV float rbf(float a) { return (float)(bf16)a; }
DEV bf16x8 as8(u32x4 v) { return __builtin_bit_cast(bf16x8, v); }
DEV bf16x16 cat16(u32x4 a, u32x4 b) { u32x8 v = {a.x, a.y, a.z, a.w, b.x, b.y, b.z, b.w}; return __builtin_bit_cast(bf16x16, v); }
DEV f32x4 mfma16(u32x4 a, u32x4 b, f32x4 c) { return __builtin_amdgcn_mfma_f32_16x16x32_bf16(as8(a), as8(b), c, 0, 0, 0); }
DEV f32x4 mfma8(u32x4 a, u32x4 b0, u32x4 b1, f32x4 c, int idx) { return __builtin_amdgcn_smfmac_f32_16x16x64_bf16(as8(a), cat16(b0, b1), c, idx, 0, 0); }
DEV f32x2 fold8(f32x4 d) { return (f32x2){d[0], d[1]} + (f32x2){d[2], d[3]}; }
DEV u32x4 ld16c(rsrc_t r, unsigned v, unsigned s) { return __builtin_amdgcn_raw_buffer_load_b128(r, v, s, SC1); }
DEV u32x2 ld8c(rsrc_t r, unsigned v, unsigned s) { return __builtin_amdgcn_raw_buffer_load_b64(r, v, s, SC1); }
DEV unsigned ld4c(rsrc_t r, unsigned v, unsigned s) { return __builtin_amdgcn_raw_buffer_load_b32(r, v, s, SC1); }
DEV void st16c(u32x4 d, rsrc_t r, unsigned v, unsigned s) {
  __builtin_amdgcn_raw_buffer_store_b128(d, r, v, s, SC1);
  asm volatile("s_nop 1" ::"v"(d) : "memory");
}
DEV void st8(u32x2 d, rsrc_t r, unsigned v, unsigned s) { __builtin_amdgcn_raw_buffer_store_b64(d, r, v, s, 0); }
DEV void st8c(u32x2 d, rsrc_t r, unsigned v, unsigned s) { __builtin_amdgcn_raw_buffer_store_b64(d, r, v, s, SC1); }
DEV void st4c(unsigned d, rsrc_t r, unsigned v, unsigned s) { __builtin_amdgcn_raw_buffer_store_b32(d, r, v, s, SC1); }
DEV unsigned umax(unsigned a, unsigned b) { return a > b ? a : b; }
DEV unsigned max4(u32x4 v) { return umax(umax(umax(v.x, v.y), v.z), v.w); }
DEV bool any_unwritten(unsigned m, int& budget) {
  const unsigned thr = --budget >= 0 ? SENT - 1u : SENT;
  return __builtin_amdgcn_ballot_w64(m > thr) != 0;
}
#define FENCE() asm volatile("" ::: "memory")
#define BAR_LDS() asm volatile("s_waitcnt lgkmcnt(0)\n\ts_barrier" ::: "memory")
#define BAR_ALL() asm volatile("s_waitcnt vmcnt(0) lgkmcnt(0)\n\ts_barrier" ::: "memory")
#define WAIT_BAR(n) asm volatile("s_waitcnt vmcnt(%0) lgkmcnt(0)\n\ts_barrier" ::"n"(n) : "memory")
#define DRAIN() asm volatile("s_waitcnt vmcnt(0)" ::: "memory")
#define SCHED() __builtin_amdgcn_sched_barrier(0)
constexpr u32x4 MARK = {SENT, SENT, SENT, SENT};
DEV float vmax(float a, float b) { float d; asm("v_max_f32 %0, %1, %2" : "=v"(d) : "v"(a), "v"(b)); return d; }
DEV float max_halves(float v) {
  auto b = __builtin_amdgcn_permlane32_swap(__float_as_uint(v), __float_as_uint(v), false, false);
  return vmax(__uint_as_float(b[0]), __uint_as_float(b[1]));
}
DEV float max_rows(float v) {
  auto a = __builtin_amdgcn_permlane16_swap(__float_as_uint(v), __float_as_uint(v), false, false);
  return max_halves(vmax(__uint_as_float(a[0]), __uint_as_float(a[1])));
}
DEV float sum_rows(float v) {
  auto a = __builtin_amdgcn_permlane16_swap(__float_as_uint(v), __float_as_uint(v), false, false);
  v = __uint_as_float(a[0]) + __uint_as_float(a[1]);
  auto b = __builtin_amdgcn_permlane32_swap(__float_as_uint(v), __float_as_uint(v), false, false);
  return __uint_as_float(b[0]) + __uint_as_float(b[1]);
}
#define RED8(op, v) asm volatile("s_nop 1\n\t" op " %0, %0, %0 quad_perm:[1,0,3,2] row_mask:0xf bank_mask:0xf\n\ts_nop 1\n\t" op " %0, %0, %0 quad_perm:[2,3,0,1] row_mask:0xf bank_mask:0xf\n\ts_nop 1\n\t" op " %0, %0, %0 row_half_mirror row_mask:0xf bank_mask:0xf" : "+v"(v))
DEV float dpp_xor1(float v) { return __uint_as_float(__builtin_amdgcn_update_dpp(0u, __float_as_uint(v), 0xB1, 0xf, 0xf, true)); }
DEV unsigned atomic_inc(unsigned char* ws, int line) {
  return __hip_atomic_fetch_add((unsigned*)(ws + OFF_CTRL + line * 128), 1u, __ATOMIC_RELAXED, __HIP_MEMORY_SCOPE_AGENT);
}
DEV unsigned wait_lines(const Ctx& c, int first, int n, unsigned want, int nap) {
  const unsigned a = OFF_CTRL + (unsigned)(first + (c.lane < n ? c.lane : 0)) * 128u;
  unsigned v;
#pragma unroll 1
  for (;;) {
    FENCE();
    v = ld4c(c.r, a, 0);
    if (__builtin_amdgcn_ballot_w64(v < want) == 0 || (c.budget -= 1 + nap) < 0) break;
    for (int i = 0; i < nap; i++) __builtin_amdgcn_s_sleep(8);
  }
  FENCE();
  return v;
}
DEV void wait_chain(const Ctx& c, int seg, bool urgent) {
  if (__builtin_amdgcn_ballot_w64(ld4c(c.r, OFF_CTRL + (unsigned)(C_PCH + c.lane) * 128u, 0) <= (unsigned)seg) != 0) {
    wait_lines(c, C_PCH + 32 + (c.role & 3), 1, seg > 0 ? seg - 1 : 0, 8);
    wait_lines(c, C_PCH + 32 + (c.role & 3), 1, seg + 1, urgent ? 0 : 2);
    wait_lines(c, C_PCH, 64, seg + 1, urgent ? 0 : 1);
  }
  FENCE();
}
DEV void ctrl_ready(const Ctx& c) {
#pragma unroll 1
  for (;;) {
    FENCE();
    if (__builtin_amdgcn_readfirstlane(ld4c(c.r, OFF_CTRL + C_READY * 128, 0)) == c.tag || --c.budget < 0) break;
  }
  FENCE();
}
DEV void set_z(const Ctx& c) {
  const unsigned v = wait_lines(c, C_POL, 8, 1, 0);
  unsigned sum = 0;
#pragma unroll
  for (int i = 0; i < 8; i++) sum += __builtin_amdgcn_readlane(v, i);
  c.zbase = comm_start(c.p.ws) + 4u * MB;
  c.zpol = sum <= 8u * 1024u;
}
struct Prog {
  unsigned v, addr;
  int kh;
  DEV void init(int first, int n, int lane, int k) { kh = k; v = 0; addr = OFF_CTRL + (unsigned)(first + (lane >> 5) * 32 + (lane & (n - 1))) * 128u; }
  DEV bool lacks(int s) const { return (unsigned)(__builtin_amdgcn_ballot_w64(v <= (unsigned)seg_idx(s, kh)) >> (32 * seg_set(s, kh))) != 0u; }
  DEV void need(const Ctx& c, int s) {
    if (lacks(s)) {
      bool again;
#pragma unroll 1
      do {
        FENCE();
        v = ld4c(c.r, addr, 0);
        asm volatile("" : "+v"(v));
        const bool more = --c.budget >= 0;
        again = lacks(s) & more;
      } while (again);
    }
    FENCE();
  }
  DEV void refresh(const Ctx& c, int s) { if (s < NSEG && lacks(s)) v = ld4c(c.r, addr, 0); }
};
DEV unsigned now() { unsigned long long t; asm volatile("s_memtime %0\n\ts_waitcnt lgkmcnt(0)" : "=s"(t)::"memory"); return (unsigned)t; }
struct Clock {
  unsigned r0, c0;
  DEV static void read(unsigned& r, unsigned& cy) {
    unsigned long long a, b;
    asm volatile("s_memrealtime %0\n\ts_memtime %1\n\ts_waitcnt lgkmcnt(0)" : "=&s"(a), "=&s"(b)::"memory");
    r = (unsigned)a * 10u; cy = (unsigned)b;
  }
  DEV void init() { read(r0, c0); r0 += 20u; }
  DEV void align() {
    unsigned r, pr, cy;
    read(pr, cy);
    r = pr;
#pragma unroll
    for (int i = 0; i < 4; i++) if (r == pr) read(r, cy);
    r0 = r + 8u; c0 = cy;
  }
  DEV static unsigned ns(unsigned cycles) { return (cycles * 445u) >> 10; }
  DEV unsigned at(unsigned cy) const { return r0 + ns(cy - c0); }
  DEV unsigned cyc(unsigned t) const {
    const int rem = (int)(t - r0);
    return c0 + (unsigned)(((rem > 4000 ? 4000 : rem < 0 ? 0 : rem) * 2355) >> 10);
  }
  DEV static void spin_cyc(unsigned ct) {
#pragma unroll 1
    for (int i = 0; i < 40; i++)
      if ((int)(now() - ct) >= 0 || (int)(now() - ct) >= 0 || (int)(now() - ct) >= 0 || (int)(now() - ct) >= 0 || (int)(now() - ct) >= 0 || (int)(now() - ct) >= 0) break;
  }
  DEV void spin(unsigned t) const { spin_cyc(cyc(t)); }
  DEV void after(unsigned cy, int dt) { align(); wait(r0 - ns(c0 - cy) + dt); }
  DEV void paced(unsigned cy, int dt, unsigned ann, int da) {
    align();
    const unsigned own = r0 - ns(c0 - cy) + dt;
    ann = __builtin_amdgcn_readfirstlane(ann);
    wait(ann != SENT && (int)(ann + da - own) > 0 ? ann + da : own);
  }
  DEV void wait(unsigned t) {
    unsigned r, cy;
    read(r, cy);
    if ((int)(r + 160u - t) < 0) {
#pragma unroll 1
      for (int i = 0; i < 256; i++) {
        const unsigned pr = r;
        read(r, cy);
        if (r == pr) read(r, cy);
        if (r == pr) read(r, cy);
        if (r != pr) { r0 = r + 8u; c0 = cy; }
        if ((int)(r + 200u - t) >= 0) break;
      }
    } else if (cy - c0 > 6000u) { r0 = r + 20u; c0 = cy; }
    spin(t);
  }
};
#define STAMP(w, role, idx) do {} while (0)
#define FDECL() do {} while (0)
#define FSTAMP(k) do {} while (0)
#define FSAVE(s) do {} while (0)
#define FDUMP() do {} while (0)
#define GSTAMP(s, k) do {} while (0)
DEV void glds16(const void* g, unsigned lds_dst) {
  unsigned keep;
  asm volatile("s_mov_b32 %0, m0\n\ts_mov_b32 m0, %2\n\ts_nop 0\n\tglobal_load_lds_dwordx4 %1, off\n\ts_mov_b32 m0, %0"
               : "=&s"(keep) : "v"(g), "s"(lds_dst) : "memory");
}
DEV void glds16s(const void* base, unsigned off, unsigned lds_dst) {
  unsigned keep;
  asm volatile("s_mov_b32 %0, m0\n\ts_mov_b32 m0, %3\n\ts_nop 0\n\tglobal_load_lds_dwordx4 %1, %2\n\ts_mov_b32 m0, %0"
               : "=&s"(keep) : "v"(off), "s"(base), "s"(lds_dst) : "memory");
}
DEV unsigned lds_addr(const void* p) { return (unsigned)(unsigned long long)(const __attribute__((address_space(3))) void*)p; }

DEV unsigned xbuf(int b) { return b == 0 ? 65536u : b == 1 ? 0u : 106496u; }
DEV int nxt3(int b) { return b == 2 ? 0 : b + 1; }
DEV void stage_x(const Ctx& c, int seg, int buf) {
  const bf16* src = c.p.x + (size_t)(seg * 32 + (c.wave >> 1) * 8) * DM + (c.wave & 1) * 320;
  const unsigned off = (unsigned)(c.lane >> 3) * (DM * 2u) + (unsigned)((c.lane & 7) ^ (c.lane >> 3)) * 16u;
  const unsigned dst = lds_addr(c.lds) + xbuf(buf) + c.wave * 5120;
#pragma unroll
  for (int i = 0; i < 5; i++) glds16s(src + i * 64, off, dst + i * 1024);
}
DEV void stage_x4(const Ctx& c, int seg, int buf) {
  Ctx o = c; o.wave = c.wave ^ 4;
  stage_x(c, seg, buf); stage_x(o, seg, buf);
}
struct XTile {
  const unsigned char *e, *o;
  DEV XTile(const Ctx& c, int buf) {
    const int n = c.lane & 15, g = c.lane >> 4;
    const unsigned b = xbuf(buf) + (n >> 3) * 10240 + (n & 7) * 128;
    e = c.lds + b + ((g ^ (n & 7)) << 4);
    o = c.lds + b + (((4 + g) ^ (n & 7)) << 4);
  }
  DEV const unsigned char* at(int rt, int ks) const { return ((ks & 1) ? o : e) + rt * 20480 + (ks >> 1) * 1024; }
};
DEV void dma_block(const Ctx& c, const bf16* src, int ld, unsigned lds_off) {
  const int q = c.lane >> 4;
#pragma unroll
  for (int i = 0; i < 4; i++) {
    const int col = ((c.lane & 15) ^ ((q << 2) | i)) << 3;
    glds16(src + (size_t)(c.wave * 16 + i * 4 + q) * ld + col, lds_addr(c.lds) + lds_off + (c.wave * 16 + i * 4) * 256);
  }
}
typedef __attribute__((address_space(3))) s16x4* lds_s16x4;
DEV void read_cols(const Ctx& c, unsigned lds_off, int col0, int cstep, u32x4* f) {
  const int g = c.lane >> 4, q = (c.lane >> 2) & 3, col = col0 + cstep * (c.lane & 3);
  const unsigned a0 = lds_addr(c.lds) + lds_off + (8 * g + q) * 256 + (((col >> 3) ^ ((q << 2) | ((2 * g) & 3))) << 4) + ((col >> 2) & 1) * 8;
  const unsigned a1 = lds_addr(c.lds) + lds_off + (8 * g + 4 + q) * 256 + (((col >> 3) ^ ((q << 2) | ((2 * g + 1) & 3))) << 4) + ((col >> 2) & 1) * 8;
#pragma unroll
  for (int ks = 0; ks < 4; ks++) {
    const u32x2 lo = __builtin_bit_cast(u32x2, __builtin_amdgcn_ds_read_tr16_b64_v4i16((lds_s16x4)(size_t)(a0 + ks * 8192)));
    const u32x2 hi = __builtin_bit_cast(u32x2, __builtin_amdgcn_ds_read_tr16_b64_v4i16((lds_s16x4)(size_t)(a1 + ks * 8192)));
    f[ks] = (u32x4){lo.x, lo.y, hi.x, hi.y};
  }
}
DEV void load_weights(const Ctx& c, u32x4 (&w)[40], const bf16* A, int lda, int stepa, const bf16* B, int ldb, int stepb, int xseg, bool plain = false) {
  auto request = [&](int j) {
    dma_block(c, A + (size_t)j * stepa, lda, (j & 1) * 65536);
    dma_block(c, B + (size_t)j * stepb, ldb, (j & 1) * 65536 + 32768);
  };
  request(0); request(1);
#pragma unroll
  for (int j = 0; j < 5; j++) {
    if (j < 4) WAIT_BAR(8); else if (xseg >= 0) WAIT_BAR(5); else WAIT_BAR(0);
    const unsigned o = (j & 1) * 65536 + (c.wave >> 2) * 32768;
    read_cols(c, o, 32 * (c.wave & 3), plain ? 4 : 8, &w[4 * j]);
    read_cols(c, o, 32 * (c.wave & 3) + (plain ? 16 : 4), plain ? 4 : 8, &w[20 + 4 * j]);
    BAR_LDS();
    if (j + 2 < 5) request(j + 2);
    if (j == 3 && xseg >= 0) stage_x(c, xseg, 0);
  }
#pragma unroll
  for (int i = 0; i < 40; i++) asm volatile("" : "+v"(w[i]));
  ctrl_ready(c);
  set_z(c);
}

DEV unsigned ring_ready(const Ctx& c) {
  wait_lines(c, C_FILL, 1, NX - 8, 0);
  set_z(c);
  return c.zpol ? 2u : 1u;
}
DEV bool unwritten2(u32x4 a, u32x4 b) { return __builtin_amdgcn_ballot_w64(umax(max4(a), max4(b)) == SENT) != 0; }
#define GAP 3
struct Looks { u32x4 b0, b1, c0, c1; DEV void retire() const { asm volatile("" ::"v"(b0), "v"(b1), "v"(c0), "v"(c1)); } };
DEV Looks gather2(const Ctx& c, unsigned base, unsigned a0, unsigned char* dst) {
  u32x4 v0 = ld16c(c.r, a0, base), v1 = ld16c(c.r, a0 + 1024u, base);
  Looks k;
  FENCE(); __builtin_amdgcn_s_sleep(GAP); FENCE();
  k.b0 = ld16c(c.r, a0, base + CM_REP); k.b1 = ld16c(c.r, a0 + 1024u, base + CM_REP);
  FENCE(); __builtin_amdgcn_s_sleep(GAP); FENCE();
  k.c0 = ld16c(c.r, a0, base + CM_REP3); k.c1 = ld16c(c.r, a0 + 1024u, base + CM_REP3);
  SCHED();
  *(u32x4*)dst = v0;
  *(u32x4*)(dst + 1024) = v1;
  SCHED();
  if (__builtin_expect(unwritten2(v0, v1), 0)) {
    *(u32x4*)dst = k.b0;
    *(u32x4*)(dst + 1024) = k.b1;
    SCHED();
    if (unwritten2(k.b0, k.b1)) {
      v0 = k.c0; v1 = k.c1;
      if (unwritten2(v0, v1)) {
#pragma unroll 1
        do {
          FENCE();
          v0 = ld16c(c.r, a0, base); v1 = ld16c(c.r, a0 + 1024u, base);
        } while (any_unwritten(umax(max4(v0), max4(v1)), c.budget));
      }
      *(u32x4*)dst = v0;
      *(u32x4*)(dst + 1024) = v1;
    }
  }
  return k;
}
DEV int key_token(int u, int m) { return 16 * ((m >> 2) & 1) + 8 * (m >> 3) + 4 * ((m >> 1) & 1) + 2 * u + (m & 1); }

template <int CLS>
DEV void role_S_loop(const Ctx& c, const u32x4 (&w)[40], int h, unsigned comm, unsigned pol) {
  c.relane();
  const int lane = c.lane, wave = c.wave, n = lane & 15, g = lane >> 4;
  const rsrc_t r = c.r;
  const float cs = 0.08838834764831845f * 1.4426950408889634f;
  Prog pr; pr.init(C_PQK + h, 1, lane, kh_qk(h));
  const int delay = TUNEV(3, 730);
  const int idx = (lane & 2) ? 0xEEEE : 0x4444;
  const unsigned ga0 = comm_off(CM_M + wave * 2048u + lane * 16u);
  const unsigned so0 = CLS == 0 ? comm_off(CM_P + h * 640u + (((lane >> 2) & 1) * 16 + 4 * g + (lane & 2) + (n >> 3)) * 16u + (lane & 1) * 8u) : CLS == 4 ? comm_off(CM_P + h * 640u + lane * 16u) : lane * 16u;
  const unsigned so1 = CLS == 0 ? ((lane & 1) ? 0x7FFFFF00u : comm_off(CM_P + h * 640u + 512u + lane * 2u)) : 1024u + n * 16u + (g - 2) * 8u;
  constexpr int NT = CLS == 3 ? 5 : 6;
  const unsigned to = (wave - 5) * 5120u + lane * 16u;
  auto tsrc = [&](int i) { return comm_off(to + i * 1024u); };
  unsigned char* const gd = c.lds + wave * 2048 + lane * 16;
  const unsigned char* const asrc = c.lds + (2 * g + ((n >> 1) & 1)) * 128 + (2 * (n >> 2) + (n & 1)) * 16;
  unsigned char* const xd = c.lds + 10240 + (wave & 3) * 1024 + ((wave >> 2) * 8 + 2 * g + 16 * (n >> 2)) * 16 + (n & 3) * 4;
  const unsigned char* const xq = c.lds + 10240 + ((n & 7) + 16 * g) * 16;
  const unsigned char* const xa = c.lds + 10240 + (2 * (n >> 2) + (n & 1) + 16 * g) * 16;
  const unsigned char* const pr_ = c.lds + 14336 + (lane ^ (lane >> 3)) * 16;
  const unsigned char* const qlo = n < 8 ? xq + wave * 1024 : c.lds + 51200;
  const unsigned char* const qhi = n < 8 ? c.lds + 51200 : xq + wave * 1024;
  unsigned char* const pd = c.lds + 14336 + wave * 1024 + (8 * (n & 7) + ((2 * g + (n >> 3)) ^ (n & 7))) * 16;
  if (CLS == 0) *(u32x4*)(c.lds + 51200) = (u32x4){0, 0, 0, 0};
  u32x4 t[NT];
  if (CLS >= 3) {
    pr.need(c, 0);
#pragma unroll
    for (int i = 0; i < NT; i++) t[i] = ld16c(r, tsrc(i), c.qkt(0, h, 0));
#pragma unroll
    for (int i = 0; i < NT; i++) *(u32x4*)(c.lds + 18432 + to + i * 1024) = t[i];
  }
  if (CLS < 2) __builtin_amdgcn_s_setprio(2);
  Clock ck;
  unsigned cgo = 0, ann = SENT;
  const int period = TUNEV(1, 1460), p_miss = TUNEV(4, 120), p_thr = TUNEV(6, 480);
  bool ahead = false;
  for (int s = 0; s < NSEG; s++) {
    FDECL();
    unsigned base = comm_base(comm, pol, s);
    unsigned o_tl = 18432 + (s & 1) * 16384 + (CLS < 2 ? 8192 + wave * 1024 + lane * 16 : 0);
    asm volatile("" : "+s"(base));
    if (CLS < 2) asm volatile("" : "+v"(o_tl));
    const unsigned char* tl = c.lds + o_tl;
    if (CLS == 3 && wave == 6) GSTAMP(s, 2);
    if (CLS == 0 && s > 0) ck.paced(cgo, delay, ann, 0);
    FSTAMP(2);
    BAR_LDS();
    u32x4 k0, k1;
    if (CLS < 2) { k0 = *(const u32x4*)tl; k1 = *(const u32x4*)(tl + 4096); }
    Looks lk;
    if (CLS < 3) lk = gather2(c, base, ga0, gd);
    unsigned tb = 0;
    if (CLS >= 3) {
      DRAIN();
      ahead = s + 1 < NSEG && !pr.lacks(s + 1);
      tb = c.qkt(ahead ? s + 1 : s, h, 0);
      asm volatile("" : "+s"(tb));
    }
    if (CLS < 2) asm volatile("" : "+v"(k0), "+v"(k1));
    FSTAMP(0);
    BAR_LDS();
    if (CLS == 2) cgo = now();
    if (CLS >= 3) {
#pragma unroll
      for (int i = 0; i < NT; i++) t[i] = ld16c(r, tsrc(i), tb);
      pr.v = ld4c(r, pr.addr, 0);
      SCHED();
    }
    f32x4 d0 = {0, 0, 0, 0}, d1 = {0, 0, 0, 0};
    {
      u32x4 a[10];
#pragma unroll
      for (int ks = 0; ks < 10; ks++) a[ks] = *(const u32x4*)(asrc + ks * 1024);
      SCHED();
#pragma unroll
      for (int ks = 0; ks < 10; ks++) {
        d0 = mfma8(a[ks], w[2 * ks], w[2 * ks + 1], d0, idx);
        d1 = mfma8(a[ks], w[20 + 2 * ks], w[21 + 2 * ks], d1, idx);
        if (CLS >= 2 && ks == 0) { SCHED(); asm volatile("s_nop 7"); SCHED(); }
      }
      SCHED();
    }
    if (CLS < 3) lk.retire();
    const f32x2 f0 = fold8(d0), f1 = fold8(d1);
    *(unsigned*)xd = pk2(f0[0], f1[0]);
    *(unsigned*)(xd + 16) = pk2(f0[1], f1[1]);
    if (CLS < 2) {
      FENCE();
      const u32x4 qa = *(const u32x4*)qlo, qb = *(const u32x4*)qhi;
      SCHED();
      *(f32x4*)pd = mfma16(k1, qb, mfma16(k0, qa, (f32x4){0, 0, 0, 0}));
    }
    FSTAMP(1);
    BAR_LDS();
    if (CLS >= 3) {
      __builtin_amdgcn_s_sleep(5);
      if (ahead) {
#pragma unroll
        for (int i = 0; i < NT; i++) *(u32x4*)(c.lds + 18432 + ((s + 1) & 1) * 16384 + to + i * 1024) = t[i];
      }
      FENCE();
      if (CLS == 4) __builtin_amdgcn_s_sleep(4);
    }
    u32x4 pt; u32x2 pm;
    if (CLS == 0) {
      u32x4 kf[4], qf[4]; f32x4 pp[4];
#pragma unroll
      for (int ke = 0; ke < 4; ke++) { kf[ke] = *(const u32x4*)(xq + 128 + ke * 1024); qf[ke] = *(const u32x4*)(xa + ke * 1024); }
#pragma unroll
      for (int ke = 0; ke < 4; ke++) pp[ke] = *(const f32x4*)(pr_ + ke * 1024);
      SCHED();
      f32x4 sm = mfma16(qf[0], kf[0], (f32x4){0, 0, 0, 0});
#pragma unroll
      for (int ke = 1; ke < 4; ke++) sm = mfma16(qf[ke], kf[ke], sm);
      SCHED();
      const f32x4 st = ((pp[0] + pp[1]) + (pp[2] + pp[3])) * cs;
      float mx = __builtin_fmaxf(__builtin_fmaxf(st[0], st[1]), st[2]);
      SCHED();
      const float mv = (n < 8 ? sm[0] : sm[1]) * cs;
      asm volatile("" ::"v"(sm));
      mx = __builtin_fmaxf(__builtin_fmaxf(mx, st[3]), mv);
      RED8("v_max_f32_dpp", mx);
      const f32x4 sd = st - mx;
      const float e4 = __builtin_amdgcn_exp2f(mv - mx);
      const f32x4 e = {__builtin_amdgcn_exp2f(sd[0]), __builtin_amdgcn_exp2f(sd[1]), __builtin_amdgcn_exp2f(sd[2]), __builtin_amdgcn_exp2f(sd[3])};
      const float e5 = dpp_xor1(e4);
      const f32x2 eh = (f32x2){e[0], e[1]} + (f32x2){e[2], e[3]};
      float sum = (e4 + eh[0]) + eh[1];
      RED8("v_add_f32_dpp", sum);
      const float inv = __builtin_amdgcn_rcpf(sum);
      const f32x4 pe = e * inv;
      const f32x2 pq = (f32x2){e4, e5} * inv;
      u32x2 o0; unsigned o1;
      asm volatile("v_cvt_pk_bf16_f32 %0, |%3|, |%4|\n\tv_cvt_pk_bf16_f32 %1, |%5|, |%6|\n\tv_cvt_pk_bf16_f32 %2, |%7|, |%8|"
                   : "=&v"(o0.x), "=&v"(o0.y), "=v"(o1) : "v"(pe[0]), "v"(pe[1]), "v"(pe[2]), "v"(pe[3]), "v"(pq[0]), "v"(pq[1]));
      st8c(o0, r, so0, base);
      st4c(o1, r, so1, base);
      FSTAMP(3);
      st8c(o0, r, so0, base + CM_REP);
      st4c(o1, r, so1, base + CM_REP);
      cgo = now();
      __builtin_amdgcn_s_sleep(3);
      FENCE();
      ann = ld4c(r, pace_addr(comm, pol, s + 1), 0);
    } else if (CLS == 2) {
      if (h == 0) {
        ck.align();
        const unsigned got = ck.r0 - Clock::ns(ck.c0 - cgo);
        ann = got + period + ((int)(got - ann) > p_thr ? p_miss : 0);
        if (lane == 0) st4c(nosent(ann), r, pace_addr(comm, pol, s + 1), 0);
      }
    } else if (CLS == 3) {
      int rel = (wave - 5) * 16 + n - key_token(0, 4 * g);
      asm volatile("" : "+v"(rel));
      const float mneg = g >= 2 ? 0.f : -3.0e38f;
      f32x4 sm = {mneg, mneg, mneg, mneg}, s0, s1;
#pragma unroll
      for (int j = 0; j < 4; j++) { s0[j] = key_token(0, j) <= rel ? 0.f : -3.0e38f; s1[j] = key_token(1, j) <= rel ? 0.f : -3.0e38f; }
      const int kt = key_token(0, n);
      const unsigned char* kp = tl + 8192 + (kt >> 4) * 4096 + ((kt & 15) + 16 * g) * 16;
#pragma unroll
      for (int ke = 0; ke < 4; ke++) {
        const u32x4 qt = *(const u32x4*)(tl + (wave - 5) * 4096 + ke * 1024 + lane * 16);
        s0 = mfma16(*(const u32x4*)(kp + ke * 1024), qt, s0);
        s1 = mfma16(*(const u32x4*)(kp + ke * 1024 + 32), qt, s1);
        sm = mfma16(*(const u32x4*)(c.lds + 10240 + ke * 1024 + lane * 16), qt, sm);
      }
      float v[12];
#pragma unroll
      for (int j = 0; j < 4; j++) { v[j] = s0[j]; v[4 + j] = s1[j]; v[8 + j] = sm[j]; }
      float mx = v[0];
#pragma unroll
      for (int j = 1; j < 12; j++) mx = __builtin_fmaxf(mx, v[j]);
      const float mc = -max_rows(mx) * cs;
#pragma unroll
      for (int j = 0; j < 12; j++) v[j] = __builtin_amdgcn_exp2f(__builtin_fmaf(v[j], cs, mc));
      const float sum = sum_rows(((v[0] + v[1]) + (v[2] + v[3])) + ((v[4] + v[5]) + (v[6] + v[7])) + ((v[8] + v[9]) + (v[10] + v[11])));
      const float inv = __builtin_amdgcn_rcpf(sum);
      pt = (u32x4){pk2(v[0] * inv, v[1] * inv), pk2(v[2] * inv, v[3] * inv), pk2(v[4] * inv, v[5] * inv), pk2(v[6] * inv, v[7] * inv)};
      pm = (u32x2){pk2(v[8] * inv, v[9] * inv), pk2(v[10] * inv, v[11] * inv)};
    }
    if (CLS >= 3) {
      if (CLS == 4) {
        if (lane == 0) st4c(s, r, OFF_CTRL + (C_PCH + h) * 128, 0);
        if ((s & 7) == 0) STAMP(7, c.role, 16 + s);
        if (s > 0 && lane < 40) {
          const unsigned pb = comm_base(comm, pol, s - 1);
          st16c(MARK, r, so0, pb); st16c(MARK, r, so0, pb + CM_REP);
        }
      }
      if (CLS == 3) {
        const unsigned sb = OFF_PT + (unsigned)((s * NH + h) * 2 + wave - 5) * PTT;
        st16c(pt, r, so0, sb);
        const auto y0 = __builtin_amdgcn_permlane16_swap(pm.x, pm.x, false, false), y1 = __builtin_amdgcn_permlane16_swap(pm.y, pm.y, false, false);
        if (g == 2) st16c((u32x4){y0[0], y1[0], y0[1], y1[1]}, r, so1, sb);
      }
      if (!ahead && s + 1 < NSEG) {
        pr.need(c, s + 1);
#pragma unroll
        for (int i = 0; i < NT; i++) t[i] = ld16c(r, tsrc(i), c.qkt(s + 1, h, 0));
#pragma unroll
        for (int i = 0; i < NT; i++) *(u32x4*)(c.lds + 18432 + ((s + 1) & 1) * 16384 + to + i * 1024) = t[i];
      }
    }
    FSAVE(s);
  }
}
DEV void role_S(const Ctx& c, int h, unsigned comm) {
  u32x4 w[40];
  load_weights(c, w, c.p.Wq + h * 128, 4096, 128 * 4096, c.p.Wk + h * 128, 4096, 128 * 4096, -1);
  STAMP(5, c.role, 6);
  const unsigned pol = ring_ready(c);
  if (c.wave == 0) role_S_loop<0>(c, w, h, comm, pol);
  else if (c.wave < 4) role_S_loop<1>(c, w, h, comm, pol);
  else if (c.wave == 4) role_S_loop<2>(c, w, h, comm, pol);
  else if (c.wave < 7) role_S_loop<3>(c, w, h, comm, pol);
  else role_S_loop<4>(c, w, h, comm, pol);
  c.relane();
  BAR_ALL();
  if (c.wave == 7 && c.lane == 0) st4c(NSEG, c.r, OFF_CTRL + (C_PCH + h) * 128, 0);
  FDUMP();
}

template <int HALF>
DEV void role_V_loop(const Ctx& c, const u32x4 (&w)[40], int h, unsigned comm, unsigned pol) {
  const int lane = c.lane, wave = c.wave, n = lane & 15, g = lane >> 4, wq = wave & 3;
  const rsrc_t r = c.r;
  const int delay = TUNEV(3, 730) + 35;
  const int idx = (lane & 2) ? 0xEEEE : 0x4444;
  const unsigned ga0 = comm_off(CM_M + wave * 2048u + lane * 16u);
  unsigned char* const gd = c.lds + wave * 2048 + lane * 16;
  const unsigned char* const asrc = c.lds + (2 * g + ((n >> 1) & 1)) * 128 + (2 * (n >> 2) + (n & 1)) * 16;
  unsigned char* const vd = c.lds + 10240 + (wq >> 1) * 1024 + (4 * g + 2 * (n & 1) + 16 * (2 * (wq & 1) + (n >> 3))) * 16 + (2 * ((n >> 2) & 1) + ((n >> 1) & 1)) * 4;
  const unsigned T0 = 8 * (g >> 1) + 2 * wq + (g & 1), T2 = 32 + 2 * wq + (g >> 1);
  const unsigned z0o = comm_off(CM_Z + T0 * 8192u + h * 256u + n * 16u), z2o = comm_off(CM_Z + T2 * 8192u + h * 256u + n * 16u + (g & 1) * 8u);
  const unsigned k0o = T0 * 256u + n * 16u, k2o = T2 * 256u + n * 16u + (g & 1) * 8u;
  Clock ck;
  unsigned cgo = 0, ann = SENT;
  for (int s = 0; s < NSEG; s++) {
    FDECL();
    unsigned base = comm_base(comm, pol, s);
    asm volatile("" : "+s"(base));
    if (HALF == 0 && wave == 3 && s > 0) ck.paced(cgo, delay, ann, 0);
    FSTAMP(2);
    BAR_LDS();
    Looks lk = {MARK, MARK, MARK, MARK};
    if (HALF == 0 || wave == 4) lk = gather2(c, base, ga0, gd);
    FSTAMP(0);
    BAR_LDS();
    u32x4 za, zb; u32x2 zc;
    if (HALF == 0) {
      f32x4 d0 = {0, 0, 0, 0}, d1 = {0, 0, 0, 0};
      u32x4 a[10];
#pragma unroll
      for (int ks = 0; ks < 10; ks++) a[ks] = *(const u32x4*)(asrc + ks * 1024);
      SCHED();
#pragma unroll
      for (int ks = 0; ks < 10; ks++) {
        d0 = mfma8(a[ks], w[2 * ks], w[2 * ks + 1], d0, idx);
        d1 = mfma8(a[ks], w[20 + 2 * ks], w[21 + 2 * ks], d1, idx);
      }
      SCHED();
      const f32x2 f0 = fold8(d0), f1 = fold8(d1);
      *(unsigned*)vd = pk2(f0[0], f1[0]);
      *(unsigned*)(vd + 16) = pk2(f0[1], f1[1]);
      FSTAMP(1);
      BAR_LDS();
      lk.retire();
    } else {
      lk.retire();
      f32x4 y[10] = {};
#pragma unroll
      for (int t = 0; t < 10; t++) asm volatile("" : "+v"(y[t]));
      FSTAMP(1);
      BAR_LDS();
      const u32x4 a0 = *(const u32x4*)(c.lds + 10240 + lane * 16), a1 = *(const u32x4*)(c.lds + 11264 + lane * 16);
      SCHED();
      unsigned z[10];
      auto fin = [&](int t) { const f32x2 f = fold8(y[t]); z[t] = nosent(pk2(f[0], f[1])); };
      auto four = [&](int t) {
        const auto q0 = __builtin_amdgcn_permlane32_swap(z[t], z[t + 2], false, false), q1 = __builtin_amdgcn_permlane32_swap(z[t + 1], z[t + 3], false, false);
        const auto q2 = __builtin_amdgcn_permlane16_swap(q0[0], q1[0], false, false), q3 = __builtin_amdgcn_permlane16_swap(q0[1], q1[1], false, false);
        return (u32x4){q2[0], q2[1], q3[0], q3[1]};
      };
#pragma unroll
      for (int t = 0; t < 10; t++) {
        const int b = (t & 1) * 20 + (t >> 1) * 4;
        y[t] = mfma8(a0, w[b], w[b + 1], y[t], idx);
        y[t] = mfma8(a1, w[b + 2], w[b + 3], y[t], idx);
        if (t > 1) fin(t - 2);
        if (t == 6) { za = four(0); st16c(za, r, z0o, base); }
        SCHED();
      }
      zb = four(4);
      st16c(zb, r, z0o + 16u * 16384u, base);
      fin(8); fin(9);
      const auto q0 = __builtin_amdgcn_permlane32_swap(z[8], z[9], false, false);
      const auto q1 = __builtin_amdgcn_permlane16_swap(q0[0], q0[1], false, false);
      zc = (u32x2){q1[0], q1[1]};
      st8c(zc, r, z2o, base);
    }
    FSTAMP(3);
    BAR_LDS();
    if (HALF == 0 && wave == 3) cgo = now();
    BAR_ALL();
    if (HALF == 0) ann = ld4c(r, pace_addr(comm, pol, s + 1), 0);
    if (HALF == 1) {
      if (wave == 7 && lane == 0) st4c(s, r, OFF_CTRL + (C_PCH + 32 + h) * 128, 0);
      const unsigned kb = OFF_ZM + (unsigned)(s * NH + h) * 10240u;
      st16c(za, r, k0o, kb); st16c(zb, r, k0o + 4096u, kb);
      const auto y0 = __builtin_amdgcn_permlane16_swap(zc.x, zc.x, false, false), y1 = __builtin_amdgcn_permlane16_swap(zc.y, zc.y, false, false);
      if (!(g & 1)) st16c((u32x4){y0[0], y1[0], y0[1], y1[1]}, r, k2o, kb);
    }
    FSAVE(s);
  }
}
DEV void role_V(const Ctx& c, int h, unsigned comm) {
  u32x4 w[40];
  load_weights(c, w, c.p.Wv + h * 128, 4096, 128 * 4096, c.p.Wo + (size_t)h * 128 * DM, DM, 128, -1, c.wave >= 4);
  STAMP(5, c.role, 6);
  const unsigned pol = ring_ready(c);
  if (c.wave >= 4) {
#pragma unroll
    for (int i = 0; i < 40; i++) {
      const u32x4 v = w[i];
      w[i] = (u32x4){__builtin_amdgcn_perm(v.z, v.x, 0x05040100u), __builtin_amdgcn_perm(v.z, v.x, 0x07060302u), __builtin_amdgcn_perm(v.w, v.y, 0x05040100u), __builtin_amdgcn_perm(v.w, v.y, 0x07060302u)};
    }
  }
#pragma unroll
  for (int i = 0; i < 40; i++) asm volatile("" : "+v"(w[i]));
  int wv = c.wave;
  asm volatile("" : "+s"(wv));
  if (wv < 4) role_V_loop<0>(c, w, h, comm, pol); else role_V_loop<1>(c, w, h, comm, pol);
  BAR_ALL();
  if (c.wave == 7 && c.lane == 0) st4c(NSEG, c.r, OFF_CTRL + (C_PCH + 32 + h) * 128, 0);
  FDUMP();
}

template <int CLS>
DEV void role_X_loop(const Ctx& c, int T, unsigned comm, unsigned pol) {
  const int lane = c.lane, wave = c.wave, n = lane & 15, g = lane >> 4;
  const rsrc_t r = c.r;
  Prog pr; pr.init(C_PVZ + 4 * wave, 4, lane, KH);
  const int margin = TUNEV(5, 1130);
  const int idx = (lane & 2) ? 0xEEEE : 0x4444;
  const unsigned po = (unsigned)(lane & 31) * 16u;
  const unsigned pa0 = comm_off(CM_P + (4 * wave + (g >> 1)) * 640u + po), pa1 = comm_off(CM_P + (4 * wave + 2 + (g >> 1)) * 640u + po);
  const unsigned pb = comm_off(CM_P + (4 * wave + g) * 640u + 512u + (2 * (n >> 2) + (n & 1)) * 16u);
  const unsigned za = comm_off(CM_Z + T * 8192u + wave * 1024u + lane * 16u);
  const unsigned mo = comm_off(CM_M + T * 256u + lane * 4u);
  float* const rw = (float*)c.lds + wave * 128 + g * 32 + n;
  const float* const rr = (const float*)c.lds + ((lane >> 2) & 7) * 16 + (lane & 3) * 4 + (lane >> 5) * 2;
  Clock ck;
  unsigned tp = 0, ann = SENT;
  const int lead = TUNEV(2, 1100);
  for (int s = 0; s + 1 < NSEG; s++) {
    FDECL();
    pr.need(c, s);
    unsigned base = comm_base(comm, pol, s), nbase = comm_base(comm, pol, s + 1);
    asm volatile("" : "+s"(base), "+s"(nbase));
    BAR_LDS();
    u32x4 zt[4];
#pragma unroll
    for (int i = 0; i < 4; i++) zt[i] = ld16c(r, lane * 16u, c.zt((s * NH + 4 * wave + i) * 40 + T));
    pr.refresh(c, s + 1);
    if ((s & 7) == 0) STAMP(7, c.role, 16 + s);
    {
      const unsigned pb_ = comm_base(comm, pol, s + 3);
      st16c(MARK, r, za, pb_);
      if (CLS == 1 && wave == 1 && lane < 48) st16c(MARK, r, comm_off(CM_M + T * 256u + n * 16u) + (g == 2 ? CM_REP3 : g * CM_REP), pb_);
    }
    if (CLS == 0) {
      GSTAMP(s, 0);
      if (s > 0) ck.paced(tp, margin, ann, lead);
    }
    FSTAMP(1);
    BAR_LDS();
    u32x4 p0 = ld16c(r, pa0, base), p1 = ld16c(r, pa1, base), pm = ld16c(r, pb, base), zm = ld16c(r, za, base);
    auto combine = [&]() {
      f32x4 acc = mfma8(p0, zt[0], zt[1], (f32x4){0, 0, 0, 0}, idx);
      acc = mfma8(p1, zt[2], zt[3], acc, idx);
      unsigned m = umax(max4(p0), max4(p1));
      SCHED();
      const f32x4 am = mfma16(pm, zm, (f32x4){0, 0, 0, 0});
      const f32x2 fa = fold8(acc);
      m = umax(m, umax(max4(pm), max4(zm)));
      SCHED();
      const f32x2 f = fa + (f32x2){am[0], am[1]};
      rw[0] = f[0];
      rw[16] = f[1];
      return m;
    };
    const unsigned m0 = combine();
    if (__builtin_expect(__builtin_amdgcn_ballot_w64(m0 == SENT) != 0, 0)) {
#pragma unroll 1
      do {
        FENCE();
        p0 = ld16c(r, pa0, base + CM_REP); p1 = ld16c(r, pa1, base + CM_REP); pm = ld16c(r, pb, base + CM_REP); zm = ld16c(r, za, base);
      } while (any_unwritten(combine(), c.budget));
    }
    DRAIN();
    FSTAMP(2);
    BAR_LDS();
    if (CLS == 0) {
      f32x2 u[8];
#pragma unroll
      for (int i = 0; i < 8; i++) u[i] = *(const f32x2*)(rr + i * 128);
      const f32x2 tt = ((u[0] + u[1]) + (u[2] + u[3])) + ((u[4] + u[5]) + (u[6] + u[7]));
      const unsigned o = nosent(pk2(tt[0], tt[1]));
      st4c(o, r, mo, nbase);
      FSTAMP(3);
      st4c(o, r, mo, nbase + CM_REP);
      st4c(o, r, mo, nbase + CM_REP3);
      ann = ld4c(r, pace_addr(comm, pol, s + 1), 0);
      tp = now();
    }
    FSAVE(s);
  }
}
DEV int xcol(int cg, int c) { return 16 * (cg >> 1) + 4 * (c >> 1) + 2 * (cg & 1) + (c & 1); }
DEV void chores(const Ctx& c, int f, int nf) {
#pragma unroll 1
  for (int f0 = f * 8 + c.wave; f0 < 240; f0 += nf * 8) {
    unsigned v[4][8];
#pragma unroll
    for (int i = 0; i < 4; i++) {
      const int k = f0 * 4 + i, pc = (k % 24) * 4 + (c.lane >> 4), hh = pc / 3, b = pc - hh * 3;
      const unsigned short* q = (const unsigned short*)(c.p.wo + (size_t)(hh * 20 + b * 8) * DM + (k / 24) * 16 + (c.lane & 15));
#pragma unroll
      for (int j = 0; j < 8; j++) { const unsigned t = q[(j >= 4 && b == 2 ? 0 : j) * DM]; v[i][j] = j >= 4 && b == 2 ? 0u : t; }
    }
#pragma unroll
    for (int i = 0; i < 4; i++)
      st16c((u32x4){v[i][0] | (v[i][1] << 16), v[i][2] | (v[i][3] << 16), v[i][4] | (v[i][5] << 16), v[i][6] | (v[i][7] << 16)}, c.r, c.lane * 16u, OFF_WO + (unsigned)(f0 * 4 + i) * KB);
  }
  BAR_ALL();
  if (c.wave == 0 && c.lane == 0) atomic_inc(c.p.ws, C_WO);
}

DEV void role_X(const Ctx& c, int T, unsigned comm) {
  const int tid = c.wave * 64 + c.lane;
  const int F = T - T / 5 - 1;
  if (T % 5 == 0) {
    const int k = T / 5;
    const unsigned cal = comm + (unsigned)(k & 1) * 2u * MB + (126u - (k >> 1)) * 16384u;
    if (c.wave < 2) st4c(1u, c.r, tid * 128u, cal);
    BAR_ALL();
    unsigned* sl = (unsigned*)c.lds;
    unsigned b0 = 0xFFFFFFFFu, b1 = 0xFFFFFFFFu, acc = 0;
#pragma unroll 1
    for (int i = 0; i < (c.wave < CALW ? CALN : 0); i++) {
      const unsigned t0 = now();
      acc += ld4c(c.r, ((i ^ c.wave) & 1) * 8192u + (c.wave * 16 + (i >> 1) * 2) * 128u, cal);
      asm volatile("" : "+v"(acc));
      const unsigned d = now() - t0;
      if ((i ^ c.wave) & 1) b1 = d < b1 ? d : b1; else b0 = d < b0 ? d : b0;
    }
    if (c.lane == 0) { sl[c.wave] = b0; sl[8 + c.wave] = b1; }
    ctrl_ready(c);
    BAR_LDS();
    if (tid == 0) {
      for (int i = 1; i < 8; i++) { b0 = min(b0, sl[i]); b1 = min(b1, sl[8 + i]); }
      int e = max(-300, min(300, (int)(b0 - b1)));
      if ((k ^ (xcc_id() >> 2)) & 1) e = -e;
      st4c(1024 + e, c.r, OFF_CTRL + (C_POL + k) * 128, 0);
    }
    BAR_LDS();
  } else {
    u32x4 v[10];
#pragma unroll
    for (int i = 0; i < 10; i++) v[i] = *(const u32x4*)(c.p.x + (size_t)((i * (NX - 8) + F) * 512 + tid) * 8);
    for (int u = F * 512 + tid; u < 202 * 1024; u += (NX - 8) * 512) {
      const int blk = u >> 10, pg = blk >= 102;
      u32x4 val = MARK;
      const unsigned lo = ((unsigned)blk & 3u) * 8192u + ((unsigned)u & 511u) * 16u;
      if ((blk < 8 || blk == 48 || blk == 49) && lo < 10240u) {
        const int q = lo >> 4;
        const unsigned* m = (const unsigned*)(c.p.mem0 + (q & 7) * DM + xcol(q >> 3, 0));
        val = (u32x4){nosent(m[0]), nosent(m[2]), nosent(m[4]), nosent(m[6])};
      }
      st16c(val, c.r, (unsigned)pg * 2u * MB + (unsigned)(u - pg * 102 * 1024) * 16u, comm);
    }
#pragma unroll
    for (int i = 0; i < 10; i++) asm volatile("" ::"v"(v[i]));
    ctrl_ready(c);
    BAR_ALL();
    if (tid == 0) atomic_inc(c.p.ws, C_FILL);
    chores(c, F, NX - 8);
  }
  STAMP(5, c.role, 6);
  const unsigned pol = ring_ready(c);
  if (c.wave == 0) role_X_loop<0>(c, T, comm, pol);
  else role_X_loop<1>(c, T, comm, pol);
  FDUMP();
}

template <int N, int D, class A, class F>
DEV void piped(A addr, F use) {
  u32x4 b[D];
#pragma unroll
  for (int i = 0; i < D; i++) b[i] = *(const u32x4*)addr(i);
  SCHED();
#pragma unroll
  for (int i = 0; i < N; i++) {
    use(i, b[i % D]);
    if (i + D < N) b[i % D] = *(const u32x4*)addr(i + D);
    SCHED();
  }
}

struct XRing {
  u32x4 b[8];
  DEV void preload(const Ctx& c, int bf) {
    const XTile xb(c, bf);
#pragma unroll
    for (int i = 0; i < 8; i++) b[i] = *(const u32x4*)xb.at(0, i);
    SCHED();
  }
  template <bool SWAP>
  DEV void run(const Ctx& c, int bf, const u32x4 (&w)[40], f32x4 (&d)[2][2]) {
    const XTile xb(c, bf);
#pragma unroll
    for (int i = 0; i < 40; i++) {
#pragma unroll
      for (int t = 0; t < 2; t++) {
        const u32x4 &fa = SWAP ? b[i % 8] : w[20 * t + i % 20], &fb = SWAP ? w[20 * t + i % 20] : b[i % 8];
        if (i % 20) asm volatile("v_mfma_f32_16x16x32_bf16 %0, %1, %2, %0" : "+v"(d[i / 20][t]) : "v"(fa), "v"(fb));
        else asm volatile("s_nop 1\n\tv_mfma_f32_16x16x32_bf16 %0, %1, %2, 0" : "=&v"(d[i / 20][t]) : "v"(fa), "v"(fb));
      }
      if (i + 8 < 40) b[i % 8] = *(const u32x4*)xb.at((i + 8) / 20, (i + 8) % 20);
      SCHED();
    }
    asm volatile("s_nop 7" : "+v"(d[0][0]), "+v"(d[0][1]), "+v"(d[1][0]), "+v"(d[1][1]));
  }
};
template <int HALF, class S, class B, class D>
DEV void stream(const Ctx& c, XRing& x, int cnt, S seg, B body, D done) {
  stage_x(c, seg(1), 1);
  stage_x(c, seg(2), 2);
  x.preload(c, 0);
  body(0, 0, [] {});
  BAR_ALL();
  done(1);
  x.preload(c, 1);
  for (int j = 1, bf = 1; j < cnt; j++, bf = nxt3(bf)) {
    const int nb = nxt3(bf), fb = nxt3(nb);
    if (HALF == 0) {
      body(j, bf, [] { DRAIN(); });
      x.preload(c, nb);
      BAR_LDS();
      done(j);
    } else {
      stage_x4(c, seg(j + 2 < cnt ? j + 2 : j), fb);
      body(j, bf, [] { BAR_ALL(); });
      x.preload(c, nb);
    }
  }
}

template <int HALF>
DEV void role_QK_loop(const Ctx& c, const u32x4 (&w)[40], int h, int set, int cnt) {
  const int lane = c.lane, wave = c.wave;
  unsigned used = 0;
  int first = 1;
  XRing x;
  stream<HALF>(c, x, cnt, [&](int j) { return seg_of(set, j, kh_qk(h)); }, [&](int j, int bf, auto mid) {
    const int s = seg_of(set, j, kh_qk(h));
    f32x4 d[2][2];
    x.run<false>(c, bf, w, d);
    mid();
    if (s >= 32 && used + 31 < (unsigned)s) used = __builtin_amdgcn_readfirstlane(wait_lines(c, C_PCH + h, 1, s - 31, 1));
    const unsigned b = c.qkt(s, h, HALF) + (wave & 3) * 1024u;
#pragma unroll
    for (int rt = 0; rt < 2; rt++)
      st16c((u32x4){pk2(d[rt][0][0], d[rt][1][0]), pk2(d[rt][0][1], d[rt][1][1]), pk2(d[rt][0][2], d[rt][1][2]), pk2(d[rt][0][3], d[rt][1][3])}, c.r, lane * 16u, b + rt * 4096u);
  }, [&](int k) {
    if (wave == 0 && lane == 0) st4c(k, c.r, OFF_CTRL + (C_PQK + set * 32 + h) * 128, 0);
    STAMP(1, c.role, 16 + k - first); first = 0;
  });
}
DEV void role_QK(const Ctx& c, int h, int set) {
  u32x4 w[40];
  load_weights(c, w, c.p.Wq + h * 128, 4096, 128 * 4096, c.p.Wk + h * 128, 4096, 128 * 4096, set);
  STAMP(5, c.role, 6);
  BAR_ALL();
  const int cnt = set ? kh_qk(h) : NSEG - kh_qk(h);
  if (c.wave < 4) role_QK_loop<0>(c, w, h, set, cnt); else role_QK_loop<1>(c, w, h, set, cnt);
  BAR_ALL();
  if (c.wave == 0 && c.lane == 0) st4c(cnt, c.r, OFF_CTRL + (C_PQK + set * 32 + h) * 128, 0);
}

template <int HALF>
DEV void role_VZ_loop(const Ctx& c, const u32x4 (&w)[40], int h, int set, int cnt) {
  const int lane = c.lane, wave = c.wave, n = lane & 15, g = lane >> 4;
  stage_x(c, seg_of(set, 1), 1);
  stage_x(c, seg_of(set, 2), 2);
  if (HALF == 0) {
    const unsigned vo = 40960 + ((n >> 1) & 1) * 4096 + wave * 1024 + (8 * (n >> 3) + 2 * ((n >> 2) & 1) + (n & 1) + 16 * g) * 16;
    XRing x;
    *(unsigned*)(c.lds + 57344) = 0;
    x.preload(c, 0);
    for (int j = 0, bf = 0; j < cnt; j++, bf = nxt3(bf)) {
      f32x4 d[2][2];
      x.run<false>(c, bf, w, d);
#pragma unroll
      for (int rt = 0; rt < 2; rt++)
        *(u32x4*)(c.lds + vo + (j & 1) * 8192 + rt * 64) = (u32x4){pk2(d[rt][0][0], d[rt][0][1]), pk2(d[rt][0][2], d[rt][0][3]), pk2(d[rt][1][0], d[rt][1][1]), pk2(d[rt][1][2], d[rt][1][3])};
      if (j == 0) BAR_ALL();
      x.preload(c, nxt3(bf));
      if (j > 0) BAR_LDS();
      if (j > 2 && wave == 0 && lane == 0) st4c(j - 1, c.r, OFF_CTRL + (C_PVZ + set * 32 + h) * 128, 0);
      if (j > 0) STAMP(1, c.role, 16 + j - 1);
    }
  } else {
    BAR_ALL();
    __builtin_amdgcn_s_setprio(1);
    for (int j = 0, bf = 0; j < cnt; j++, bf = nxt3(bf)) {
      if (j > 0) stage_x4(c, seg_of(set, j + 3 < cnt ? j + 3 : j), bf);
      u32x4 va[8];
#pragma unroll
      for (int i = 0; i < 8; i++) va[i] = *(const u32x4*)(c.lds + 40960 + (j & 1) * 8192 + i * 1024 + lane * 16);
      SCHED();
      const int b = (seg_of(set, j) * NH + h) * 40 + 2 * (wave & 3);
      f32x4 z[10][2];
      auto put = [&](int t) {
        st16c((u32x4){pk2(z[t][0][0], z[t][0][1]), pk2(z[t][0][2], z[t][0][3]), pk2(z[t][1][0], z[t][1][1]), pk2(z[t][1][2], z[t][1][3])}, c.r, lane * 16u, c.zt(b + (t >> 1) * 8 + (t & 1)));
      };
#pragma unroll
      for (int t = 0; t < 10; t++) {
        const int wb = (t & 1) * 20 + (t >> 1) * 4;
        z[t][0] = z[t][1] = (f32x4){0, 0, 0, 0};
#pragma unroll
        for (int ke = 0; ke < 4; ke++) {
          z[t][0] = mfma16(va[ke], w[wb + ke], z[t][0]);
          z[t][1] = mfma16(va[4 + ke], w[wb + ke], z[t][1]);
        }
        if (t > 0) put(t - 1);
        SCHED();
      }
      if (j == 0) {
        __builtin_amdgcn_s_setprio(0);
        put(9);
        stage_x4(c, seg_of(set, 3), 0);
        asm volatile("s_waitcnt vmcnt(10)" ::: "memory");
        if (lane == 0 && __hip_atomic_fetch_add((unsigned*)(c.lds + 57344), 1u, __ATOMIC_RELAXED, __HIP_MEMORY_SCOPE_WORKGROUP) == 3u)
          st4c(1, c.r, OFF_CTRL + (C_PVZ + set * 32 + h) * 128, 0);
        BAR_ALL();
      } else {
        if (j + 1 < cnt) WAIT_BAR(9);
        put(9);
      }
    }
  }
}
DEV void role_VZ(const Ctx& c, int h, int set) {
  u32x4 w[40];
  load_weights(c, w, c.p.Wv + h * 128, 4096, 128 * 4096, c.p.Wo + (size_t)h * 128 * DM, DM, 128, set, c.wave >= 4);
  STAMP(5, c.role, 6);
  BAR_ALL();
  const int cnt = set ? KH : NSEG - KH;
  if (c.wave < 4) role_VZ_loop<0>(c, w, h, set, cnt); else role_VZ_loop<1>(c, w, h, set, cnt);
  BAR_ALL();
  if (c.wave == 0 && c.lane == 0) st4c(cnt, c.r, OFF_CTRL + (C_PVZ + set * 32 + h) * 128, 0);
}

constexpr float CSA = 0.22360679774997896f * 1.4426950408889634f;
DEV void role_qkv(const Ctx& c, int a) {
  const int lane = c.lane, wave = c.wave, n = lane & 15, g = lane >> 4;
  const rsrc_t r = c.r;
  const int cgp = a & 7, s0 = (a >> 3) * QSEG, s1 = s0 + QSEG < NSEG ? s0 + QSEG : NSEG;
  const int sl = cgp * 8 + wave;
  u32x4 w[40];
  load_weights(c, w, c.p.wqkv + cgp * 256, 1920, 128 * 1920, c.p.wqkv + cgp * 256 + (cgp < 7 ? 128 : 0), 1920, 128 * 1920, s0, cgp >= 5);
  if (sl == 60) {
#pragma unroll
    for (int ks = 0; ks < 20; ks++) w[ks] = *(const u32x4*)(c.p.gw + ks * 32 + g * 8);
  }
  BAR_ALL();
#pragma unroll
  for (int i = 0; i < 20; i++) asm volatile("" : "+v"(w[i]));
  STAMP(5, c.role, 6);
  const int c8 = (sl < 20 ? sl : sl - 20) * 4 + g, grp5 = c8 % 5, hq = (c8 / 5) * 2 + (grp5 > 2);
  const bool left = grp5 == 2;
  const unsigned o0 = (unsigned)hq * 64u * QAT + (left ? 1024u + n * 16u : (unsigned)((sl < 20 ? n : 16 * (n >> 3) + (n & 7)) + 32 * (grp5 == 1 || grp5 == 4)) * 16u);
  const unsigned o1 = o0 + (left ? 64u * QAT : sl < 20 ? 256u : 128u);
  XRing x;
  auto seg = [&](int j) { return s0 + j; };
  auto none = [&](int k) { STAMP(1, c.role, 16 + k); };
  auto qk = [&](int j, int bf, auto mid) {
    f32x4 d[2][2];
    x.run<false>(c, bf, w, d);
    mid();
    if (sl == 60) {
      const float z = rbf(g & 1 ? d[1][0][0] : d[0][0][0]);
      if (g < 2) st4c(__float_as_uint(rbf(1.f / (1.f + __expf(-z)))), r, OFF_GATE + (unsigned)((s0 + j) * 32 + lane) * 4u, 0);
    } else if (sl < 40) {
      const float sc = sl < 20 ? CSA : 1.f;
      u32x2 p[2][2];
#pragma unroll
      for (int i = 0; i < 4; i++) p[i >> 1][i & 1] = (u32x2){pk2(d[i >> 1][i & 1][0] * sc, d[i >> 1][i & 1][1] * sc), pk2(d[i >> 1][i & 1][2] * sc, d[i >> 1][i & 1][3] * sc)};
      const u32x2 u = left ? p[1][0] : p[0][1], v = left ? p[0][1] : p[1][0];
      const unsigned b = (sl < 20 ? OFF_QA : OFF_KA) + (unsigned)(s0 + j) * QAT;
      st16c((u32x4){p[0][0].x, p[0][0].y, u.x, u.y}, r, o0, b);
      st16c((u32x4){v.x, v.y, p[1][1].x, p[1][1].y}, r, o1, b);
    }
  };
  auto vv = [&](int j, int bf, auto mid) {
    f32x4 d[2][2];
    x.run<true>(c, bf, w, d);
    mid();
#pragma unroll
    for (int t = 0; t < 2; t++)
      st16c((u32x4){pk2(d[0][t][0], d[0][t][1]), pk2(d[0][t][2], d[0][t][3]), pk2(d[1][t][0], d[1][t][1]), pk2(d[1][t][2], d[1][t][3])}, r,
            (unsigned)(g * 640 + t * 16 + n) * 16u, OFF_VA + (unsigned)(s0 + j) * 40960u + (unsigned)(sl - 40) * 512u);
  };
  if (sl < 40 || sl >= 60) { if (wave < 4) stream<0>(c, x, s1 - s0, seg, qk, none); else stream<1>(c, x, s1 - s0, seg, qk, none); }
  else { if (wave < 4) stream<0>(c, x, s1 - s0, seg, vv, none); else stream<1>(c, x, s1 - s0, seg, vv, none); }
  BAR_ALL();
  if (wave == 0 && lane == 0) atomic_inc(c.p.ws, C_QKVDONE);
}

DEV int take_ticket(const Ctx& c, int line) {
  c.relane();
  unsigned* slot = (unsigned*)(c.lds + 160 * 1024 - 16);
  BAR_LDS();
  if (c.wave == 0 && c.lane == 0) *slot = atomic_inc(c.p.ws, line);
  BAR_ALL();
  return __builtin_amdgcn_readfirstlane(*slot);
}

DEV f32x16 qk32(u32x4 k0, u32x2 k1, u32x4 q0, u32x2 q1) {
  f32x16 z;
#pragma unroll
  for (int i = 0; i < 16; i++) z[i] = 0;
  z = __builtin_amdgcn_mfma_f32_32x32x16_bf16(as8(k0), as8(q0), z, 0, 0, 0);
  asm volatile("s_nop 11" : "+v"(z));
  return __builtin_amdgcn_mfma_f32_32x32x8bf16_1k(__builtin_bit_cast(s16x4, k1), __builtin_bit_cast(s16x4, q1), z, 0, 0, 0);
}
DEV u32x2 split_neg(float m) {
  const float h1 = rbf(m), h2 = rbf(m - h1), h3 = rbf((m - h1) - h2);
  return (u32x2){pk2(-h1, -h2), pk2(-h3, 0.f)};
}
DEV float max16(const f32x16& s) {
  float mx = s[0];
#pragma unroll
  for (int i = 1; i < 16; i++) mx = __builtin_fmaxf(mx, s[i]);
  return max_halves(mx);
}
DEV u32x4 exp8(const f32x16& s, int o, float sh) {
  float e[8];
#pragma unroll
  for (int j = 0; j < 8; j++) e[j] = __builtin_amdgcn_exp2f(s[o + j] - sh);
  return (u32x4){pk2(e[0], e[1]), pk2(e[2], e[3]), pk2(e[4], e[5]), pk2(e[6], e[7])};
}
DEV f32x16 pv32(u32x4 v, u32x4 p, const f32x16& o) { return __builtin_amdgcn_mfma_f32_32x32x16_bf16(as8(v), as8(p), o, 0, 0, 0); }

DEV void attention(const Ctx& c) {
  int u = take_ticket(c, C_QATT);
  if (u >= 256) return;
  const int lane = c.lane, wave = c.wave, tid = wave * 64 + lane, q = lane & 31, hf = lane >> 5;
  const rsrc_t r = c.r;
  constexpr int CH = 8 * KVT;
  const int k1o = hf ? 1280 + 320 : 1024 + (8 * (q >> 4) + (q & 7)) * 16 + ((q >> 3) & 1) * 8, vo = 1280 + hf * 384 + (q < 20 ? q : 20) * 16;
  unsigned* const slot = (unsigned*)(c.lds + 2 * CH);
  if (tid < 64) *(u32x4*)(c.lds + (tid >> 2) * KVT + 1280 + (tid & 3) * 384 + 320) = (u32x4){0x3F803F80u, 0x3F803F80u, 0x3F803F80u, 0x3F803F80u};
  auto ksrc = [](int i) { return (unsigned)i * 16u; };
  auto kdst = [](int i) { return (unsigned)(i / 80) * KVT + (unsigned)(i % 80) * 16u; };
  auto vsrc = [](int i) { return (unsigned)((i / 20) * 640 + i % 20) * 16u; };
  auto vdst = [](int i) { return (unsigned)(i / 80) * KVT + 1280u + (unsigned)((i / 20) & 3) * 384u + (unsigned)(i % 20) * 16u; };
  const int ex = 512 + (tid & 127);
  const unsigned g0 = ksrc(tid), g1 = vsrc(tid), g2 = (wave & 2) ? vsrc(ex) : ksrc(ex);
  const unsigned l0 = kdst(tid), l1 = vdst(tid), l2 = (wave & 2) ? vdst(ex) : kdst(ex);
  u32x4 st0, st1, st2, nq0; u32x2 nq1;
  auto qblock = [](int u) { return ((u >> 5) + S0 / 8) & 7; };
  auto fetch = [&](int h, int ch) {
    const unsigned kb = OFF_KA + (unsigned)(h * 64 + ch * 8) * QAT, vb = OFF_VA + (unsigned)ch * 327680u + (unsigned)h * 320u;
    st0 = ld16c(r, g0, kb); st1 = ld16c(r, g1, vb);
    if (wave < 4) st2 = ld16c(r, g2, (wave & 2) ? vb : kb);
  };
  auto fetch_unit = [&](int u) {
    const unsigned qo = OFF_QA + (unsigned)((u & 31) * 64 + qblock(u) * 8 + wave) * QAT;
    nq0 = ld16c(r, lane * 16u, qo); nq1 = ld8c(r, 1024u + (q & 15) * 16u + (q >> 4) * 8u, qo);
    fetch(u & 31, 0);
  };
  auto stash = [&](int b) {
    unsigned char* nb = c.lds + b * CH;
    *(u32x4*)(nb + l0) = st0; *(u32x4*)(nb + l1) = st1;
    if (wave < 4) *(u32x4*)(nb + l2) = st2;
  };
  fetch_unit(u);
  stash(0);
  BAR_LDS();
  int prev = -1;
#pragma unroll 1
  while (u < 256) {
    const int h = u & 31, qb = qblock(u);
    const u32x4 q0 = nq0;
    u32x2 q1 = hf ? (u32x2){0, 0} : nq1;
    unsigned tk = 0;
    int nu = 256;
    float m = max16(qk32(*(const u32x4*)(c.lds + lane * 16), *(const u32x2*)(c.lds + k1o), q0, q1));
    if (hf) q1 = split_neg(m);
    f32x16 o;
#pragma unroll
    for (int i = 0; i < 16; i++) o[i] = 0;
#pragma unroll 1
    for (int ch = 0; ch < 8; ch++) {
      if (ch == 5 && tid == 0) tk = atomic_inc(c.p.ws, C_QATT);
      if (ch == 7) nu = __builtin_amdgcn_readfirstlane(*slot);
      if (ch < 7) fetch(h, ch + 1);
      else fetch_unit(nu < 256 ? nu : u);
      const unsigned char* kb = c.lds + (ch & 1) * CH;
      const f32x16 keep = o;
      {
        u32x4 k0 = *(const u32x4*)(kb + lane * 16); u32x2 k1 = *(const u32x2*)(kb + k1o);
        f32x16 sc = qk32(k0, k1, q0, q1);
        k0 = *(const u32x4*)(kb + KVT + lane * 16); k1 = *(const u32x2*)(kb + KVT + k1o);
#pragma unroll
        for (int t = 0; t < 8; t++) {
          const u32x4 v0 = *(const u32x4*)(kb + t * KVT + vo), v1 = *(const u32x4*)(kb + t * KVT + vo + 768);
          f32x16 sn;
#pragma unroll
          for (int i = 0; i < 16; i++) sn[i] = 0;
          float e[8];
          SCHED();
#pragma unroll
          for (int j = 0; j < 4; j++) e[j] = __builtin_amdgcn_exp2f(sc[j]);
          SCHED();
          if (t + 1 < 8) sn = __builtin_amdgcn_mfma_f32_32x32x16_bf16(as8(k0), as8(q0), sn, 0, 0, 0);
          SCHED();
#pragma unroll
          for (int j = 4; j < 8; j++) e[j] = __builtin_amdgcn_exp2f(sc[j]);
          const u32x4 pa = {pk2(e[0], e[1]), pk2(e[2], e[3]), pk2(e[4], e[5]), pk2(e[6], e[7])};
#pragma unroll
          for (int j = 0; j < 2; j++) e[j] = __builtin_amdgcn_exp2f(sc[8 + j]);
          SCHED();
          if (t + 1 < 8) sn = __builtin_amdgcn_mfma_f32_32x32x8bf16_1k(__builtin_bit_cast(s16x4, k1), __builtin_bit_cast(s16x4, q1), sn, 0, 0, 0);
          if (t + 2 < 8) { k0 = *(const u32x4*)(kb + (t + 2) * KVT + lane * 16); k1 = *(const u32x2*)(kb + (t + 2) * KVT + k1o); }
          SCHED();
#pragma unroll
          for (int j = 2; j < 5; j++) e[j] = __builtin_amdgcn_exp2f(sc[8 + j]);
          SCHED();
          o = pv32(v0, pa, o);
          SCHED();
#pragma unroll
          for (int j = 5; j < 8; j++) e[j] = __builtin_amdgcn_exp2f(sc[8 + j]);
          const u32x4 pb = {pk2(e[0], e[1]), pk2(e[2], e[3]), pk2(e[4], e[5]), pk2(e[6], e[7])};
          SCHED();
          o = pv32(v1, pb, o);
          if (t + 1 < 8) sc = sn;
        }
      }
      if (__builtin_amdgcn_ballot_w64(!(o[12] < 1.0e9f)) != 0) {
        o = keep;
#pragma unroll 1
        for (int t = 0; t < 8; t++) {
          const unsigned char* kt = kb + t * KVT;
          const f32x16 sc = qk32(*(const u32x4*)(kt + lane * 16), *(const u32x2*)(kt + k1o), q0, q1);
          const float mn = m + __builtin_fmaxf(max16(sc), 0.f), sh = mn - m, al = __builtin_amdgcn_exp2f(-sh);
#pragma unroll
          for (int i = 0; i < 16; i++) o[i] *= al;
          m = mn;
          if (hf) q1 = split_neg(m);
          o = pv32(*(const u32x4*)(kt + vo), exp8(sc, 0, sh), o);
          o = pv32(*(const u32x4*)(kt + vo + 768), exp8(sc, 8, sh), o);
        }
      }
      stash((ch + 1) & 1);
      if (ch == 5 && tid == 0) *slot = tk;
      BAR_ALL();
      if (ch == 0 && prev >= 0 && tid == 0) atomic_inc(c.p.ws, C_AT + prev);
    }
    const float inv = 1.0f / o[12];
    const unsigned ob = OFF_OF + (unsigned)(qb * 8 + wave) * 49152u + (unsigned)h * 768u, ol = (q >> 4) * 24576u + (q & 15) * 16u;
    const auto x0 = __builtin_amdgcn_permlane32_swap(pk2(o[0] * inv, o[1] * inv), pk2(o[4] * inv, o[5] * inv), false, false);
    const auto x1 = __builtin_amdgcn_permlane32_swap(pk2(o[2] * inv, o[3] * inv), pk2(o[6] * inv, o[7] * inv), false, false);
    st16c((u32x4){x0[0], x1[0], x0[1], x1[1]}, r, ol + hf * 256u, ob);
    if (!hf) st16c((u32x4){pk2(o[8] * inv, o[9] * inv), pk2(o[10] * inv, o[11] * inv), 0, 0}, r, ol + 512u, ob);
    STAMP(5, 256, u);
    prev = qb;
    u = nu;
  }
  BAR_ALL();
  if (tid == 0) atomic_inc(c.p.ws, C_AT + prev);
}

template <int MODE>
DEV void final_unit(const Ctx& c, int seg, int cb, int prev = -1) {
  c.relane();
  const int lane = c.lane, wave = c.wave, tid = wave * 64 + lane, n = lane & 15, g = lane >> 4;
  const rsrc_t r = c.r;
  const int T = cb * 8 + wave;
  if (wave == 0) {
    if (MODE != 1) wait_lines(c, C_AT + (seg >> 3), 1, 32, 1);
    if (MODE == 0) wait_lines(c, C_PVZ + seg_set(seg) * 32, 32, seg_idx(seg) + 1, 1);
    if (MODE == 1) wait_chain(c, seg, false);
    if (MODE == 2) wait_lines(c, C_BD + seg, 1, 5, 1);
  }
  BAR_LDS();
  f32x4 a0 = {0, 0, 0, 0}, a1 = {0, 0, 0, 0}, b0 = {0, 0, 0, 0}, b1 = {0, 0, 0, 0};
  float g0 = 0, g1 = 0;
  u32x4 zt[32], bp;
  constexpr int ZE = MODE == 0 ? 14 : 0;
  if (MODE != 1) {
    u32x4 t[6], wf[24];
#pragma unroll
    for (int i = 0; i < 6; i++) t[i] = ld16c(r, tid * 16u, OFF_OF + (unsigned)seg * 49152u + i * 8192u);
#pragma unroll
    for (int i = 0; i < 24; i++) wf[i] = ld16c(r, lane * 16u + (i & 3) * 1024u, OFF_WO + (unsigned)T * 24576u + (i >> 2) * 4096u);
#pragma unroll
    for (int h = 0; h < ZE; h++) zt[h] = ld16c(r, lane * 16u, c.zt((seg * NH + h) * 40 + T));
    if (MODE == 2) bp = ld16c(r, lane * 16u, OFF_BP + (unsigned)(seg * 40 + T) * KB);
    g0 = __uint_as_float(ld4c(r, n * 4u, OFF_GATE + seg * 128u)); g1 = __uint_as_float(ld4c(r, n * 4u + 64u, OFF_GATE + seg * 128u));
    SCHED();
#pragma unroll
    for (int i = 0; i < 6; i++) *(u32x4*)(c.lds + i * 8192 + tid * 16) = t[i];
    BAR_LDS();
    piped<48, 4>([&](int i) { return c.lds + ((i & 1) * 24 + (i >> 1)) * 1024 + lane * 16; }, [&](int i, const u32x4& of) {
      if (i & 1) a1 = mfma16(wf[i >> 1], of, a1); else a0 = mfma16(wf[i >> 1], of, a0);
    });
    asm volatile("" : "+v"(a0), "+v"(a1));
  }
  if (MODE != 2) {
    if (MODE == 0) {
#pragma unroll
      for (int h = ZE; h < 32; h++) zt[h] = ld16c(r, lane * 16u, c.zt((seg * NH + h) * 40 + T));
      if (wave == 0) wait_chain(c, seg, false);
      BAR_LDS();
    }
    unsigned char* pl = c.lds + 49152;
    u32x4 zm[8];
    {
      u32x4 t[10];
#pragma unroll
      for (int i = 0; i < 10; i++) t[i] = ld16c(r, tid * 16u, OFF_PT + (unsigned)seg * 81920u + i * 8192u);
      if (MODE == 1) {
#pragma unroll
        for (int h = 0; h < 32; h++) zt[h] = ld16c(r, lane * 16u, c.zt((seg * NH + h) * 40 + T));
      }
#pragma unroll
      for (int i = 0; i < 8; i++) zm[i] = ld16c(r, g * 10240u + n * 16u, OFF_ZM + (unsigned)(seg * NH + 4 * i) * 10240u + T * 256u);
      SCHED();
#pragma unroll
      for (int i = 0; i < 10; i++) *(u32x4*)(pl + i * 8192 + tid * 16) = t[i];
    }
    if (MODE == 1) WAIT_BAR(40); else BAR_LDS();
    if (MODE == 1 && prev >= 0 && tid == 0) atomic_inc(c.p.ws, C_BD + prev);
    piped<64, 8>([&](int i) { return pl + i * PTT + lane * 16; }, [&](int i, const u32x4& pf) {
      if (i & 1) b1 = mfma16(zt[i >> 1], pf, b1); else b0 = mfma16(zt[i >> 1], pf, b0);
    });
    piped<16, 8>([&](int i) { return pl + ((4 * (i >> 1) + g) * 2 + (i & 1)) * PTT + 1024 + n * 16; }, [&](int i, const u32x4& pf) {
      if (i & 1) b1 = mfma16(zm[i >> 1], pf, b1); else b0 = mfma16(zm[i >> 1], pf, b0);
    });
    bp = (u32x4){pk2(b0[0], b0[1]), pk2(b0[2], b0[3]), pk2(b1[0], b1[1]), pk2(b1[2], b1[3])};
  }
  if (MODE == 1) {
    st16c(bp, r, lane * 16u, OFF_BP + (unsigned)(seg * 40 + T) * KB);
    return;
  }
  const bf16x8 bb = as8(bp);
  const float h0 = rbf(1.f - g0), h1 = rbf(1.f - g1);
  float y0[4], y1[4];
#pragma unroll
  for (int j = 0; j < 4; j++) {
    y0[j] = rbf(g0 * rbf(a0[j])) + rbf(h0 * (float)bb[j]);
    y1[j] = rbf(g1 * rbf(a1[j])) + rbf(h1 * (float)bb[4 + j]);
  }
  const rsrc_t ro = make_rsrc(c.p.out, 2048 * DM * 2);
  const unsigned o = (unsigned)((seg * 32 + n) * DM + T * 16 + 4 * g) * 2u;
  st8((u32x2){pk2(y0[0], y0[1]), pk2(y0[2], y0[3])}, ro, o, 0);
  st8((u32x2){pk2(y1[0], y1[1]), pk2(y1[2], y1[3])}, ro, o + 16 * DM * 2, 0);
}

DEV void tail_unit(const Ctx& c, int seg, int rt, int T0) {
  c.relane();
  const int lane = c.lane, wave = c.wave, n = lane & 15, g = lane >> 4;
  const rsrc_t r = c.r;
  if (wave == 0) { wait_lines(c, C_AT + (seg >> 3), 1, 32, 1); wait_lines(c, C_PVZ + seg_set(seg) * 32, 32, seg_idx(seg) + 1, 1); }
  BAR_LDS();
  u32x4 of[3], wf[3][2], zt[4][2];
#pragma unroll
  for (int i = 0; i < 3; i++) {
    const int ks = wave + 8 * i;
    of[i] = ld16c(r, lane * 16u, OFF_OF + (unsigned)((seg * 2 + rt) * 24 + ks) * KB);
    wf[i][0] = ld16c(r, lane * 16u, OFF_WO + (unsigned)(T0 * 24 + ks) * KB);
    wf[i][1] = ld16c(r, lane * 16u, OFF_WO + (unsigned)(T0 * 24 + 24 + ks) * KB);
  }
#pragma unroll
  for (int i = 0; i < 8; i++) zt[i >> 1][i & 1] = ld16c(r, lane * 16u, c.zt((seg * NH + 4 * wave + (i >> 1)) * 40 + T0 + (i & 1)));
  const float gt = __uint_as_float(ld4c(r, n * 4u, OFF_GATE + (unsigned)(seg * 32 + rt * 16) * 4u));
  f32x4* part = (f32x4*)c.lds;
  auto total = [&](int o) {
    f32x4 u[8];
#pragma unroll
    for (int i = 0; i < 8; i++) u[i] = part[o + (i * 2 + (wave & 1)) * 64 + lane];
    return ((u[0] + u[1]) + (u[2] + u[3])) + ((u[4] + u[5]) + (u[6] + u[7]));
  };
#pragma unroll
  for (int t = 0; t < 2; t++) part[(wave * 2 + t) * 64 + lane] = mfma16(wf[2][t], of[2], mfma16(wf[1][t], of[1], mfma16(wf[0][t], of[0], (f32x4){0, 0, 0, 0})));
  BAR_LDS();
  f32x4 a = total(0);
#pragma unroll
  for (int j = 0; j < 4; j++) a[j] = rbf(gt * rbf(a[j]));
  asm volatile("" : "+v"(a), "+v"(zt[0][0]), "+v"(zt[3][1]));
  if (wave == 7) { wait_lines(c, C_PCH + 32 + (c.role & 3), 1, seg - 1, 8); wait_lines(c, C_PCH + 32 + (c.role & 3), 1, seg, 1); wait_lines(c, C_PCH, 64, seg + 1, 0); }
  BAR_LDS();
  u32x4 pf[4], zm[2];
#pragma unroll
  for (int i = 0; i < 4; i++) pf[i] = ld16c(r, lane * 16u, OFF_PT + (unsigned)((seg * NH + 4 * wave + i) * 2 + rt) * PTT);
  const u32x4 pm = ld16c(r, g * 2u * PTT + 1024u + n * 16u, OFF_PT + (unsigned)((seg * NH + 4 * wave) * 2 + rt) * PTT);
#pragma unroll
  for (int t = 0; t < 2; t++) zm[t] = ld16c(r, g * 10240u + n * 16u, OFF_ZM + (unsigned)(seg * NH + 4 * wave) * 10240u + (unsigned)(T0 + t) * 256u);
#pragma unroll
  for (int t = 0; t < 2; t++) {
    f32x4 b = {0, 0, 0, 0};
#pragma unroll
    for (int i = 0; i < 4; i++) b = mfma16(zt[i][t], pf[i], b);
    part[1024 + (wave * 2 + t) * 64 + lane] = mfma16(zm[t], pm, b);
  }
  BAR_LDS();
  if (wave < 2) {
    const f32x4 b = total(1024);
    const float hg = rbf(1.f - gt);
    st8((u32x2){pk2(a[0] + rbf(hg * rbf(b[0])), a[1] + rbf(hg * rbf(b[1]))), pk2(a[2] + rbf(hg * rbf(b[2])), a[3] + rbf(hg * rbf(b[3])))},
         make_rsrc(c.p.out, 2048 * DM * 2), (unsigned)((seg * 32 + rt * 16 + n) * DM + (T0 + wave) * 16 + 4 * g) * 2u, 0);
  }
}
constexpr int S1 = NSEG - 2;
#define PLACE 0
__host__ __device__ inline void place(int grp, int slot, int& kind, int& id) {
  const int ns[4] = {16, 16, 0, 0}, nv[4] = {16, 16, 0, 0}, nx[4] = {0, 0, 20, 20};
  int s0 = 0, v0 = 0, x0 = 0, a0 = 0;
  for (int k = 0; k < 4; k++) if (k < grp) { s0 += ns[k]; v0 += nv[k]; x0 += nx[k]; a0 += 32 - ns[k] - nv[k] - nx[k]; }
  int cs = 0, cv = 0, cx = 0;
  for (int k = 0; k < 4; k++) if (k == grp) { cs = ns[k]; cv = nv[k]; cx = nx[k]; }
  if (slot < cs) { kind = 0; id = s0 + slot; }
  else if (slot < cs + cv) { kind = 1; id = v0 + slot - cs; }
  else if (slot < cs + cv + cx) { kind = 2; id = x0 + slot - cs - cv; }
  else { kind = 3; id = a0 + slot - cs - cv - cx; }
}
__global__ void __launch_bounds__(512, 2) main_kernel(Params p) {
  extern __shared__ __attribute__((aligned(16))) unsigned char lds[];
  const int lane = threadIdx.x & 63, wave = __builtin_amdgcn_readfirstlane(threadIdx.x >> 6);
  Ctx c{p, make_rsrc(p.ws, WS_USED), lds, lane, wave, 0, SPIN_CAP, 0, 0, 0};
#define LSTAMP(k) do {} while (0)
  LSTAMP(0); LSTAMP(1); LSTAMP(2); LSTAMP(3); LSTAMP(4);
  const unsigned comm = comm_start(p.ws);
  unsigned* sl = (unsigned*)(lds + 160 * 1024 - 128);
  const unsigned seed = __builtin_amdgcn_readfirstlane(ld4c(c.r, OFF_CTRL + C_SEED * 128, 0));
  c.tag = seed + 0x9E3779B9u;
  if (blockIdx.x == 0 && threadIdx.x >= 64 && threadIdx.x - 62 < C_LINES) st4c(0u, c.r, OFF_CTRL + (threadIdx.x - 62) * 128u, 0);
  if (wave == 0) {
    auto claim = [&](unsigned role) {
      unsigned old = 0;
      if (lane == 0) old = __hip_atomic_exchange((unsigned*)(p.ws + OFF_CLAIM + role * 128), c.tag, __ATOMIC_RELAXED, __HIP_MEMORY_SCOPE_AGENT);
      return __builtin_amdgcn_readfirstlane(old) != c.tag;
    };
    unsigned role = xcc_id() * 32 + (blockIdx.x >> 3);
    const bool mine = claim(role);
    if (lane == 0) st4c(c.tag, c.r, OFF_CLAIM + 32768u + blockIdx.x * 4u, 0);
    if (!mine) {
#pragma unroll 1
      for (;;) {
        FENCE();
        const u32x4 v = ld16c(c.r, OFF_CLAIM + 32768u + lane * 16u, 0);
        if (__builtin_amdgcn_ballot_w64(v.x != c.tag || v.y != c.tag || v.z != c.tag || v.w != c.tag) == 0 || --c.budget < 0) break;
      }
#pragma unroll 1
      for (role = 0; role < 256u && !claim(role); role++) {}
    }
    if (lane == 0) sl[0] = role;
  }
  if (blockIdx.x == 0) BAR_ALL(); else BAR_LDS();
  if (blockIdx.x == 0 && threadIdx.x == 64) st4c(c.tag, c.r, OFF_CTRL + C_READY * 128, 0);
  const int role = __builtin_amdgcn_readfirstlane(sl[0]);
  const int grp = role >> 5, slot = role & 31;
  if (grp > 7) return;
  c.role = role;
  STAMP(5, c.role, 1);
  int kind = 3, id = 0;
  if (grp < 4) place(grp, slot, kind, id);
  if (kind == 0) { role_S(c, id, comm); return; }
  if (kind == 1) { role_V(c, id, comm); return; }
  if (kind == 2) { role_X(c, id, comm); return; }
  if (grp < 4) {
    role_qkv(c, id);
    STAMP(5, c.role, 2);
    if (wave == 0) { wait_lines(c, C_PCH + 36, 1, S1 - 6, 32); wait_lines(c, C_QKVDONE, 1, NA, 1); wait_lines(c, C_WO, 1, NX - 8, 1); }
  } else {
  if (slot & 8) role_VZ(c, (grp - 4) * 8 + (slot & 7), slot >> 4);
  else role_QK(c, (grp - 4) * 8 + (slot & 7), slot >> 4);
  STAMP(5, c.role, 2);
  const bool follow = grp >= 4 && slot >= 16 && slot < 16 + NBX;
  if (follow) {
    int prev = -1;
#pragma unroll 1
    for (int u = (slot - 16) * 4 + grp - 4; u < S0 * 5; u += 4 * NBX) {
      final_unit<1>(c, u / 5, u % 5, prev);
      prev = u / 5;
      STAMP(5, 258, u);
    }
    BAR_ALL();
    if (wave == 0 && c.lane == 0) atomic_inc(p.ws, C_BD + prev);
  }
  if (wave == 0) { wait_lines(c, C_QKVDONE, 1, NA, 1); wait_lines(c, C_WO, 1, NX - 8, 1); }
  STAMP(0, c.role, 3);
  attention(c);
  LSTAMP(1);
  STAMP(5, c.role, 4);
  for (;;) {
    const int u = take_ticket(c, C_QA);
    if (u >= S0 * 5) break;
    final_unit<2>(c, u / 5, u % 5);
    STAMP(5, 257, u);
  }
  LSTAMP(2);
  for (;;) {
    const int u = take_ticket(c, C_QFIN) + S0 * 5;
    if (u >= S1 * 5) break;
    final_unit<0>(c, u / 5, u % 5);
    STAMP(5, 257, u);
  }
  LSTAMP(3);
  }
  for (;;) {
    const int u = take_ticket(c, C_QTAIL);
    if (u >= (NSEG - S1) * 40) break;
    tail_unit(c, S1 + u / 40, u & 1, ((u % 40) >> 1) * 2);
    if (u == (NSEG - S1) * 40 - 1 && wave == 0 && c.lane == 0) st4c(seed + 1u, c.r, OFF_CTRL + C_SEED * 128, 0);
    STAMP(5, 257, 320 + u);
  }
  LSTAMP(4);
  STAMP(5, c.role, 5);
}

extern "C" void kernel_launch(void* const* d_in, const int* in_sizes, int n_in, void* d_out, int out_size, void* d_ws, size_t ws_size,
                              hipStream_t stream) {
  Params p;
  p.x = (const bf16*)d_in[0]; p.wqkv = (const bf16*)d_in[1]; p.wo = (const bf16*)d_in[2]; p.mem0 = (const bf16*)d_in[4];
  p.Wq = (const bf16*)d_in[5]; p.Wk = (const bf16*)d_in[6]; p.Wv = (const bf16*)d_in[7]; p.Wo = (const bf16*)d_in[8];
  p.gw = (const bf16*)d_in[10]; p.out = (bf16*)d_out; p.ws = (unsigned char*)d_ws;
  (void)hipFuncSetAttribute((const void*)main_kernel, hipFuncAttributeMaxDynamicSharedMemorySize, 160 * 1024);
  main_kernel<<<256, 512, 160 * 1024, stream>>>(p);
}
```
